```python
import torch

def _softmax(x, axis=-1):
    return torch.softmax(x, dim=axis)
def _gelu(x):
    return torch.nn.functional.gelu(x, approximate="tanh")
def _silu(x):
    return torch.nn.functional.silu(x)
def _sigmoid(x):
    return torch.sigmoid(x)
def _layernorm(x, axis=-1, eps=1e-5):
    m = x.mean(dim=axis, keepdim=True)
    v = x.var(dim=axis, keepdim=True, unbiased=False)
    return (x - m) / torch.sqrt(v + eps)
def _scatter_add(tgt, idx, src, axis=0):
    return tgt.clone().index_add_(axis, idx, src)
def _segment_sum(src, seg, n):
    out = torch.zeros((n,) + src.shape[1:], dtype=src.dtype)
    return out.index_add_(0, seg, src)
def _pad(x, pw):
    flat = tuple(v for pair in reversed(pw) for v in pair)
    return torch.nn.functional.pad(x, flat)

kernel_name = 'concat_heads__gqa_block_cached__gqa_block_cached__near_ridge'

def setup_inputs():

    _w = (lambda a: (lambda t: t.to(torch.bfloat16) if t.is_floating_point() else t)(torch.as_tensor(a)))
    def _A_setup():
        B, Sq, D = 8, 80, 448
        Hq, Hkv, Sk, Dh = 16, 8, 1024, 256
        assert Hq % Hkv == 0, (Hq, Hkv)
        return {
            "x": _w((lambda *s: torch.randn(*s, dtype=torch.bfloat16) * 0.8)(B, Sq, D)),
            "k_cache": _w((lambda *s: torch.randn(*s, dtype=torch.bfloat16) * 0.8)(B, Hkv, Sk, Dh)),
            "v_cache": _w((lambda *s: torch.randn(*s, dtype=torch.bfloat16) * 0.05)(B, Hkv, Sk, Dh)),
            "wq": _w((lambda *s: torch.randn(*s, dtype=torch.bfloat16) * 0.8)(D, Hq * Dh) * (D ** -0.5)),
            "wo": _w((lambda *s: torch.randn(*s, dtype=torch.bfloat16) * 0.05)(Hq * Dh, D) * ((Hq * Dh) ** -0.5)),
        }
    _A_in = _A_setup()

    def _B_setup():
        B, Sq, D = 8, 80, 448
        Hq, Hkv, Sk, Dh = 16, 8, 1024, 256
        assert Hq % Hkv == 0, (Hq, Hkv)
        return {
            "x": _w((lambda *s: torch.randn(*s, dtype=torch.bfloat16) * 0.8)(B, Sq, D)),
            "k_cache": _w((lambda *s: torch.randn(*s, dtype=torch.bfloat16) * 0.8)(B, Hkv, Sk, Dh)),
            "v_cache": _w((lambda *s: torch.randn(*s, dtype=torch.bfloat16) * 0.05)(B, Hkv, Sk, Dh)),
            "wq": _w((lambda *s: torch.randn(*s, dtype=torch.bfloat16) * 0.8)(D, Hq * Dh) * (D ** -0.5)),
            "wo": _w((lambda *s: torch.randn(*s, dtype=torch.bfloat16) * 0.05)(Hq * Dh, D) * ((Hq * Dh) ** -0.5)),
        }
    _B_in = _B_setup()

    return {
        "A_x": _A_in["x"],
        "A_k_cache": _A_in["k_cache"],
        "A_v_cache": _A_in["v_cache"],
        "A_wq": _A_in["wq"],
        "A_wo": _A_in["wo"],
        "B_x": _B_in["x"],
        "B_k_cache": _B_in["k_cache"],
        "B_v_cache": _B_in["v_cache"],
        "B_wq": _B_in["wq"],
        "B_wo": _B_in["wo"],
    }

def reference(A_x, A_k_cache, A_v_cache, A_wq, A_wo, B_x, B_k_cache, B_v_cache, B_wq, B_wo):
    def _A_ref(x, k_cache, v_cache, wq, wo):
        B, Sq, D = x.shape
        Hq, Dh = 16, 256
        Hkv = k_cache.shape[1]
        rep = Hq // Hkv
        q = (lambda x, *d: x.permute(*d))((x @ wq).reshape(B, Sq, Hq, Dh), 0, 2, 1, 3)
        k = (lambda x, n, axis: torch.repeat_interleave(x, n, dim=axis))(k_cache, rep, axis=1)
        v = (lambda x, n, axis: torch.repeat_interleave(x, n, dim=axis))(v_cache, rep, axis=1)
        scores = torch.einsum("bhqd,bhkd->bhqk", q, k) * (Dh ** -0.5)
        p = _softmax(scores, axis=-1)
        o = torch.einsum("bhqk,bhkd->bhqd", p, v)
        return (lambda x, *d: x.permute(*d))(o, 0, 2, 1, 3).reshape(B, Sq, Hq * Dh) @ wo

    def _B_ref(x, k_cache, v_cache, wq, wo):
        B, Sq, D = x.shape
        Hq, Dh = 16, 256
        Hkv = k_cache.shape[1]
        rep = Hq // Hkv
        q = (lambda x, *d: x.permute(*d))((x @ wq).reshape(B, Sq, Hq, Dh), 0, 2, 1, 3)
        k = (lambda x, n, axis: torch.repeat_interleave(x, n, dim=axis))(k_cache, rep, axis=1)
        v = (lambda x, n, axis: torch.repeat_interleave(x, n, dim=axis))(v_cache, rep, axis=1)
        scores = torch.einsum("bhqd,bhkd->bhqk", q, k) * (Dh ** -0.5)
        p = _softmax(scores, axis=-1)
        o = torch.einsum("bhqk,bhkd->bhqd", p, v)
        return (lambda x, *d: x.permute(*d))(o, 0, 2, 1, 3).reshape(B, Sq, Hq * Dh) @ wo

    a = _A_ref(A_x, A_k_cache, A_v_cache, A_wq, A_wo)
    b = _B_ref(B_x, B_k_cache, B_v_cache, B_wq, B_wo)
    return torch.concatenate([a, b], axis=-1)
```

```cpp
#include <hip/hip_runtime.h>
#include <hip/hip_bf16.h>
#include <stdint.h>

#define KV_NT 1
#define W_NT 0

typedef __bf16 bf16x8 __attribute__((ext_vector_type(8)));
typedef __bf16 bf16x2 __attribute__((ext_vector_type(2)));
typedef short s16x4 __attribute__((ext_vector_type(4)));
typedef short s16x8 __attribute__((ext_vector_type(8)));
typedef float f32x4 __attribute__((ext_vector_type(4)));
typedef float f32x2 __attribute__((ext_vector_type(2)));
typedef unsigned u32x4 __attribute__((ext_vector_type(4)));
typedef unsigned u32x2 __attribute__((ext_vector_type(2)));
#define LAS __attribute__((address_space(3)))
typedef LAS char* lptr;

constexpr int NB = 8, SQ = 80, DM = 448, HQ = 16, HKV = 8, SK = 1024, DH = 256;
constexpr int NTILE = SK / 64;
constexpr int KPIECE = 1056, KSLOT = 8 * KPIECE, KRING = 2 * KSLOT;
constexpr int LDS_V = 4 * KRING, VSLOT = 8192, VRING = 16384;
constexpr int LDS_X = LDS_V + 3 * VRING;
constexpr int LDS_P = LDS_X + 20480, PBUF = 10240;
constexpr int LDS_REF = LDS_X + 40960;
constexpr int LDS_CHG = LDS_REF + 2560;
constexpr int LDS_L = LDS_CHG + 32;
constexpr int LDS_TOTAL = 163840;
static_assert(LDS_L + 1280 <= LDS_TOTAL, "lds");

constexpr float SCL = 0.0625f * 1.4426950408889634f;
constexpr float THR = 10.f;
constexpr float NEG = -1.0e30f;
constexpr int NBLK = 70;

#define WAIT_VM(n) asm volatile("s_waitcnt vmcnt(" #n ")" ::: "memory")
#define WAIT_LGKM0() asm volatile("s_waitcnt lgkmcnt(0)" ::: "memory")
#define BAR() do { __builtin_amdgcn_sched_barrier(0); asm volatile("" ::: "memory"); __builtin_amdgcn_s_barrier(); asm volatile("" ::: "memory"); __builtin_amdgcn_sched_barrier(0); } while (0)

template <int NT>
__device__ __forceinline__ void dma16(unsigned m0v, unsigned voff, __amdgpu_buffer_rsrc_t srd, unsigned soff) {
  if (NT) asm volatile("s_mov_b32 m0, %0\n\ts_nop 0\n\tbuffer_load_dwordx4 %1, %2, %3 offen nt lds" :: "s"(m0v), "v"(voff), "s"(srd), "s"(soff) : "memory");
  else    asm volatile("s_mov_b32 m0, %0\n\ts_nop 0\n\tbuffer_load_dwordx4 %1, %2, %3 offen lds" :: "s"(m0v), "v"(voff), "s"(srd), "s"(soff) : "memory");
}
template <int NT>
__device__ __forceinline__ void dma_rows(unsigned lds, unsigned ve, unsigned vo, __amdgpu_buffer_rsrc_t srd, unsigned soff, unsigned pstride) {
#pragma unroll
  for (int p = 0; p < 8; ++p) dma16<NT>(lds + p * 1024, (p & 1) ? vo : ve, srd, soff + p * pstride);
}
template <int NT>
__device__ __forceinline__ void dma_k(unsigned lds, unsigned v, __amdgpu_buffer_rsrc_t srd, unsigned soff) {
#pragma unroll
  for (int p = 0; p < 8; ++p) dma16<NT>(lds + p * KPIECE, v, srd, soff + p * 1024);
}
__device__ __forceinline__ bf16x8 ld128(lptr p) { return *(LAS bf16x8*)p; }
__device__ __forceinline__ bf16x8 ldtr(lptr p) {
  s16x4 a = __builtin_amdgcn_ds_read_tr16_b64_v4i16((LAS s16x4*)p);
  s16x4 b = __builtin_amdgcn_ds_read_tr16_b64_v4i16((LAS s16x4*)(p + 512));
  return __builtin_bit_cast(bf16x8, __builtin_shufflevector(a, b, 0, 1, 2, 3, 4, 5, 6, 7));
}
__device__ __forceinline__ unsigned pk2(float a, float b) { f32x2 t = {a, b}; return __builtin_bit_cast(unsigned, __builtin_convertvector(t, bf16x2)); }
__device__ __forceinline__ u32x2 pk4(f32x4 v) { u32x2 r = {pk2(v[0], v[1]), pk2(v[2], v[3])}; return r; }
__device__ __forceinline__ f32x4 mfma(bf16x8 a, bf16x8 b, f32x4 c) { return __builtin_amdgcn_mfma_f32_16x16x32_bf16(a, b, c, 0, 0, 0); }
__device__ __forceinline__ float ex2(float x) { return __builtin_amdgcn_exp2f(x); }
__device__ __forceinline__ float red_max_g(float v) {
  auto a = __builtin_amdgcn_permlane16_swap(__float_as_uint(v), __float_as_uint(v), false, false);
  v = fmaxf(__uint_as_float(a[0]), __uint_as_float(a[1]));
  auto b = __builtin_amdgcn_permlane32_swap(__float_as_uint(v), __float_as_uint(v), false, false);
  return fmaxf(__uint_as_float(b[0]), __uint_as_float(b[1]));
}
__device__ __forceinline__ float red_sum_g(float v) {
  auto a = __builtin_amdgcn_permlane16_swap(__float_as_uint(v), __float_as_uint(v), false, false);
  v = __uint_as_float(a[0]) + __uint_as_float(a[1]);
  auto b = __builtin_amdgcn_permlane32_swap(__float_as_uint(v), __float_as_uint(v), false, false);
  return __uint_as_float(b[0]) + __uint_as_float(b[1]);
}

template <int V> struct IC { static constexpr int value = V; };
__device__ __forceinline__ int opaque(int v) { asm volatile("" : "+v"(v)); return v; }
#define SCHED_FENCE() __builtin_amdgcn_sched_barrier(0)

struct RowImg { int trb, fsw; };
__device__ __forceinline__ RowImg row_img(int lane) {
  RowImg r; r.fsw = ((lane >> 3) & 1) | (((lane >> 4) & 1) << 1);
  r.trb = (8 * (lane >> 4) + ((lane >> 2) & 3)) * 128 + 8 * (lane & 3); return r;
}
__device__ __forceinline__ unsigned row_src(int lane, int stride) {
  const int srow = lane >> 3; return srow * stride + (((lane & 7) ^ (((srow >> 1) & 1) << 1)) * 16);
}
__device__ __forceinline__ void ld_slot(bf16x8 (*dst)[4], lptr base, const RowImg& ri) {
#pragma unroll
  for (int ks = 0; ks < 2; ++ks)
#pragma unroll
    for (int u = 0; u < 4; ++u) dst[ks][u] = ldtr(base + ri.trb + 32 * (u ^ ri.fsw) + 4096 * ks);
}
__device__ __forceinline__ void out_proj(bf16x8 (*wf)[4], lptr smem, char* pdst, int lane) {
#pragma unroll
  for (int t = 0; t < 5; ++t) {
    bf16x8 of[8];
#pragma unroll
    for (int ks = 0; ks < 8; ++ks) of[ks] = ld128(smem + LDS_X + (3 - (ks >> 1)) * PBUF + ((ks & 1) * 5 + t) * 1024 + lane * 16);
    f32x4 o[4];
#pragma unroll
    for (int u = 0; u < 4; ++u) o[u] = f32x4{0.f, 0.f, 0.f, 0.f};
#pragma unroll
    for (int ks = 0; ks < 8; ++ks)
#pragma unroll
      for (int u = 0; u < 4; ++u) o[u] = mfma(wf[ks][u], of[ks], o[u]);
#pragma unroll
    for (int m = 0; m < 2; ++m) {
      const u32x2 a = pk4(o[2 * m]), c = pk4(o[2 * m + 1]);
      *(u32x4*)(pdst + (t * 2 + m) * 1024) = u32x4{a[0], a[1], c[0], c[1]};
    }
  }
}

__global__ __launch_bounds__(512, 2)
void gqa_main(const char* __restrict__ xA, const char* __restrict__ kA, const char* __restrict__ vA, const char* __restrict__ wqA, const char* __restrict__ woA,
              const char* __restrict__ xB, const char* __restrict__ kB, const char* __restrict__ vB, const char* __restrict__ wqB, const char* __restrict__ woB,
              char* __restrict__ part) {
  __shared__ __attribute__((aligned(16))) char smem_raw[LDS_TOTAL];
  const lptr smem = (lptr)smem_raw;
  const unsigned ldsb = (unsigned)(uintptr_t)smem;
  const int tid = threadIdx.x;
  const int wid = __builtin_amdgcn_readfirstlane(tid >> 6);
  const int bid = blockIdx.x, xcd = bid & 7, jj = bid >> 3;
  const int br = xcd >> 2, b = jj >> 2, kvh = 2 * (xcd & 3) + ((jj >> 1) & 1), h = 2 * kvh + (jj & 1);
  const __amdgpu_buffer_rsrc_t srdWo = __builtin_amdgcn_make_buffer_rsrc((void*)((br ? woB : woA) + (size_t)h * DH * DM * 2 + wid * 128), (short)0, DH * DM * 2 - wid * 128, 0x00020000);
  char* const pbase = part + ((size_t)((br * NB + b) * HQ + h) * NBLK + wid * 10) * 1024;
  constexpr unsigned WOSLOT = 64 * DM * 2, WOP = 8 * DM * 2;

  if (wid < 4) {
    const int w = wid;
    __builtin_amdgcn_s_setprio(1);
    const char* x = (br ? xB : xA) + (size_t)b * SQ * DM * 2;
    const __amdgpu_buffer_rsrc_t srdK = __builtin_amdgcn_make_buffer_rsrc((void*)((br ? kB : kA) + ((size_t)(b * HKV + kvh) * SK + w * 16) * (DH * 2)), (short)0, (SK - w * 16) * DH * 2, 0x00020000);
    const unsigned kring = ldsb + w * KRING;
    const int lane = tid & 63;
    const unsigned kv = (lane >> 5) * 512 + (((lane & 31) ^ (lane >> 5)) * 16);
    {
      u32x4 xr[7][3];
      unsigned xo[3];
#pragma unroll
      for (int i = 0; i < 3; ++i) { int blk = w + 4 * i; blk = blk < 10 ? blk : 9; xo[i] = ((blk % 5) * 16 + (lane & 15)) * (DM * 2) + (blk / 5) * 64 + (lane >> 4) * 16; }
#pragma unroll
      for (int j = 0; j < 7; ++j)
#pragma unroll
        for (int i = 0; i < 3; ++i)
          asm volatile("global_load_dwordx4 %0, %1, %2 offset:%3" : "=v"(xr[j][i]) : "v"(xo[i]), "s"(x), "i"(j * 128) : "memory");
      dma_k<KV_NT>(kring, kv, srdK, 0);
      dma_k<KV_NT>(kring + KSLOT, kv, srdK, 64 * DH * 2);
      if (lane < 8) *(LAS unsigned*)(smem + LDS_CHG + lane * 4) = 0;
      asm volatile("s_waitcnt vmcnt(16)"
                   : "+v"(xr[0][0]), "+v"(xr[0][1]), "+v"(xr[0][2]), "+v"(xr[1][0]), "+v"(xr[1][1]), "+v"(xr[1][2]), "+v"(xr[2][0]), "+v"(xr[2][1]), "+v"(xr[2][2]),
                     "+v"(xr[3][0]), "+v"(xr[3][1]), "+v"(xr[3][2]), "+v"(xr[4][0]), "+v"(xr[4][1]), "+v"(xr[4][2]), "+v"(xr[5][0]), "+v"(xr[5][1]), "+v"(xr[5][2]),
                     "+v"(xr[6][0]), "+v"(xr[6][1]), "+v"(xr[6][2]) :: "memory");
      const lptr xdst = smem + LDS_P + w * 1024 + lane * 16;
#define XWRITE(j) do { *(LAS u32x4*)(xdst + ((j) & 1) * PBUF) = xr[j][0]; *(LAS u32x4*)(xdst + ((j) & 1) * PBUF + 4096) = xr[j][1]; \
                       if (w < 2) *(LAS u32x4*)(xdst + ((j) & 1) * PBUF + 8192) = xr[j][2]; } while (0)
      XWRITE(0); XWRITE(1);
      WAIT_LGKM0(); BAR();
#pragma unroll
      for (int j = 0; j < 7; ++j) {
        BAR();
        if (j + 2 <= 6) { XWRITE(j + 2 <= 6 ? j + 2 : 0); WAIT_LGKM0(); }
      }
#undef XWRITE
    }
    BAR();
    bf16x8 qf[8][5];
#pragma unroll
    for (int ks = 0; ks < 8; ++ks)
#pragma unroll
      for (int t = 0; t < 5; ++t) qf[ks][t] = ld128(smem + LDS_X + (3 - (ks >> 1)) * PBUF + ((ks & 1) * 5 + t) * 1024 + lane * 16);
    WAIT_LGKM0(); BAR();
    float ref[5], l[5];
#pragma unroll
    for (int t = 0; t < 5; ++t) { ref[t] = NEG; l[t] = 0.f; }
    const lptr ka0 = smem + w * KRING + ((lane & 15) >> 1) * KPIECE + (lane & 1) * 512 + (((lane >> 4) ^ (lane & 1)) * 16);
    const lptr pw0 = smem + LDS_P + (w >> 1) * 5120 + (w & 1) * 512 + 256 * (lane >> 5) + 16 * (lane & 15) + 8 * ((lane >> 4) & 1);
    auto adopt = [&](int par) {
      const u32x4 ch = *(LAS u32x4*)(smem + LDS_CHG + par * 16);
      if (__builtin_amdgcn_readfirstlane(ch[0] | ch[1] | ch[2] | ch[3])) {
        const lptr rb = smem + LDS_REF + par * 1280 + (opaque(lane) & 15) * 4;
#pragma unroll
        for (int t = 0; t < 5; ++t) {
          float m = ref[t];
#pragma unroll
          for (int ww = 0; ww < 4; ++ww) { float r = *(LAS float*)(rb + ww * 320 + t * 64); asm volatile("" : "+v"(r)); m = fmaxf(m, ch[ww] ? r : NEG); }
          const float f = ex2(ref[t] - m);
          l[t] *= f; ref[t] = m;
          LAS u32x2* pp = (LAS u32x2*)(pw0 + par * PBUF + t * 1024);
          const u32x2 pv = *pp;
          *pp = u32x2{pk2(__uint_as_float(pv[0] << 16) * f, __uint_as_float(pv[0] & 0xffff0000u) * f), pk2(__uint_as_float(pv[1] << 16) * f, __uint_as_float(pv[1] & 0xffff0000u) * f)};
          SCHED_FENCE();
        }
        WAIT_LGKM0(); BAR();
      }
    };
    auto tile = [&](int T, auto mode) {
      WAIT_VM(8);
      const lptr ka = ka0 + (T & 1) * KSLOT;
      bf16x8 kf[8];
#pragma unroll
      for (int i = 0; i < 8; ++i) kf[i] = ld128(ka + 64 * i);
      adopt((T + 1) & 1);
      WAIT_LGKM0();
      if constexpr (decltype(mode)::value == 0) dma_k<KV_NT>(kring + (T & 1) * KSLOT, kv, srdK, (T + 2) * (64 * DH * 2));
      else { const unsigned we = row_src(opaque(lane), DM * 2); dma_rows<W_NT>(kring + (decltype(mode)::value - 1) * KSLOT, we, we ^ 64, srdWo, (decltype(mode)::value - 1) * WOSLOT, WOP); }
      f32x4 s[5];
#pragma unroll
      for (int t = 0; t < 5; ++t) s[t] = f32x4{0.f, 0.f, 0.f, 0.f};
#pragma unroll
      for (int i = 0; i < 8; ++i)
#pragma unroll
        for (int t = 0; t < 5; ++t) s[t] = mfma(kf[i], qf[i][t], s[t]);
      float mx = NEG;
#pragma unroll
      for (int t = 0; t < 5; ++t) mx = fmaxf(mx, fmaf(fmaxf(fmaxf(s[t][0], s[t][1]), fmaxf(s[t][2], s[t][3])), SCL, -ref[t]));
      unsigned changed = 0;
      if (__builtin_amdgcn_ballot_w64(mx > THR)) {
        changed = 1;
        const int ln = opaque(lane);
        const lptr rb = smem + LDS_REF + ((T & 1) * 4 + w) * 320 + (ln & 15) * 4;
#pragma unroll
        for (int t = 0; t < 5; ++t) {
          const float m = red_max_g(fmaxf(fmaxf(s[t][0], s[t][1]), fmaxf(s[t][2], s[t][3])));
          const float nr = fmaxf(ref[t], ceilf(m * SCL));
          l[t] *= ex2(ref[t] - nr); ref[t] = nr;
          if (ln < 16) *(LAS float*)(rb + t * 64) = nr;
          SCHED_FENCE();
        }
      }
      const lptr pw = pw0 + (T & 1) * PBUF;
#pragma unroll
      for (int t = 0; t < 5; ++t) {
        f32x4 p;
#pragma unroll
        for (int r = 0; r < 4; ++r) p[r] = ex2(fmaf(s[t][r], SCL, -ref[t]));
        l[t] += (p[0] + p[1]) + (p[2] + p[3]);
        *(LAS u32x2*)(pw + t * 1024) = pk4(p);
      }
      if (lane == 0) *(LAS unsigned*)(smem + LDS_CHG + ((T & 1) * 4 + w) * 4) = changed;
      WAIT_LGKM0(); BAR();
    };
    for (int T = 0; T < NTILE - 2; ++T) tile(T, IC<0>{});
    tile(NTILE - 2, IC<1>{});
    tile(NTILE - 1, IC<2>{});
    adopt(1);
    const int ln = opaque(lane);
#pragma unroll
    for (int t = 0; t < 5; ++t) { const float ls = red_sum_g(l[t]); if (ln < 16) *(LAS float*)(smem + LDS_L + (w * 80 + t * 16 + ln) * 4) = ls; }
    WAIT_LGKM0(); BAR();
    bf16x8 wf[8][4];
    const lptr wring = smem + w * KRING;
    const RowImg ri = row_img(ln);
    const unsigned we = row_src(ln, DM * 2);
    WAIT_VM(8);
    ld_slot(wf + 0, wring, ri);
    WAIT_LGKM0();
    dma_rows<W_NT>(kring, we, we ^ 64, srdWo, 2 * WOSLOT, WOP);
    WAIT_VM(8);
    ld_slot(wf + 2, wring + KSLOT, ri);
    WAIT_LGKM0();
    dma_rows<W_NT>(kring + KSLOT, we, we ^ 64, srdWo, 3 * WOSLOT, WOP);
    BAR();
    WAIT_VM(8);
    ld_slot(wf + 4, wring, ri);
    WAIT_VM(0);
    ld_slot(wf + 6, wring + KSLOT, ri);
    out_proj(wf, smem, pbase + ln * 16, ln);
  } else {
    const int v = wid - 4;
    const int lane = tid & 63;
    const __amdgpu_buffer_rsrc_t srdQ = __builtin_amdgcn_make_buffer_rsrc((void*)((br ? wqB : wqA) + (h * DH + v * 64) * 2), (short)0, DM * HQ * DH * 2 - (h * DH + v * 64) * 2, 0x00020000);
    const __amdgpu_buffer_rsrc_t srdV = __builtin_amdgcn_make_buffer_rsrc((void*)((br ? vB : vA) + (size_t)(b * HKV + kvh) * SK * DH * 2 + v * 128), (short)0, SK * DH * 2 - v * 128, 0x00020000);
    const unsigned vring = ldsb + LDS_V + v * VRING;
    const unsigned wqe = row_src(lane, HQ * DH * 2), ve = row_src(lane, DH * 2), vo = ve ^ 64;
    dma_rows<W_NT>(vring, wqe, wqe ^ 64, srdQ, 0, 8 * HQ * DH * 2);
    dma_rows<W_NT>(vring + VSLOT, wqe, wqe ^ 64, srdQ, 64 * HQ * DH * 2, 8 * HQ * DH * 2);
    const RowImg ri = row_img(lane);
    const lptr wring = smem + LDS_V + v * VRING;
    const lptr pr0 = smem + LDS_P + lane * 16;
    f32x4 acc[4][5];
#pragma unroll
    for (int u = 0; u < 4; ++u)
#pragma unroll
      for (int t = 0; t < 5; ++t) acc[u][t] = f32x4{0.f, 0.f, 0.f, 0.f};
    BAR();
    {
      bf16x8 pf[2][2][5];
#pragma unroll
      for (int ks = 0; ks < 2; ++ks)
#pragma unroll
        for (int t = 0; t < 5; ++t) pf[0][ks][t] = ld128(pr0 + (ks * 5 + t) * 1024);
#pragma unroll
      for (int j = 0; j < 7; ++j) {
        WAIT_LGKM0(); BAR();
        if (j == 6 && v == 3) WAIT_VM(0); else WAIT_VM(8);
        bf16x8 af[2][4];
        ld_slot(af, wring + (j & 1) * VSLOT, ri);
        WAIT_LGKM0();
        if (j + 2 <= 6) dma_rows<W_NT>(vring + (j & 1) * VSLOT, wqe, wqe ^ 64, srdQ, (j + 2) * (64 * HQ * DH * 2), 8 * HQ * DH * 2);
        else if (v < 3) dma_rows<KV_NT>(vring + (j & 1) * VSLOT, ve, vo, srdV, (j - 5) * (64 * DH * 2), 8 * DH * 2);
        if (j < 6) {
#pragma unroll
          for (int ks = 0; ks < 2; ++ks)
#pragma unroll
            for (int t = 0; t < 5; ++t) pf[(j + 1) & 1][ks][t] = ld128(pr0 + ((j + 1) & 1) * PBUF + (ks * 5 + t) * 1024);
        }
#pragma unroll
        for (int ks = 0; ks < 2; ++ks)
#pragma unroll
          for (int u = 0; u < 4; ++u)
#pragma unroll
            for (int t = 0; t < 5; ++t) acc[u][t] = mfma(af[ks][u], pf[j & 1][ks][t], acc[u][t]);
      }
    }
    const lptr xo = smem + LDS_X + (3 - v) * PBUF + 256 * (lane >> 5) + 16 * (lane & 15) + 8 * ((lane >> 4) & 1);
#pragma unroll
    for (int u = 0; u < 4; ++u)
#pragma unroll
      for (int t = 0; t < 5; ++t) { *(LAS u32x2*)(xo + (u >> 1) * 5120 + (u & 1) * 512 + t * 1024) = pk4(acc[u][t]); acc[u][t] = f32x4{0.f, 0.f, 0.f, 0.f}; }
    WAIT_LGKM0(); BAR();
    BAR();
    if (v == 3) {
      dma_rows<KV_NT>(vring + VSLOT, ve, vo, srdV, 0, 8 * DH * 2);
      dma_rows<KV_NT>(vring, ve, vo, srdV, 64 * DH * 2, 8 * DH * 2);
    }
    float mo[5];
#pragma unroll
    for (int t = 0; t < 5; ++t) mo[t] = NEG;
    BAR();
    auto tile = [&](int T, auto mode) {
      constexpr int MODE = decltype(mode)::value;
      if (MODE == 2 && v == 3) WAIT_VM(0); else WAIT_VM(8);
      const int vs = ((T + 1) & 1) * VSLOT;
      const lptr pr = pr0 + (T & 1) * PBUF;
      bf16x8 af[2][4], pf[2][5];
      const u32x4 ch = *(LAS u32x4*)(smem + LDS_CHG + (T & 1) * 16);
      ld_slot(af, wring + vs, ri);
      if (__builtin_amdgcn_readfirstlane(ch[0] | ch[1] | ch[2] | ch[3])) {
        const lptr rb = smem + LDS_REF + (T & 1) * 1280 + (opaque(lane) & 15) * 4;
#pragma unroll
        for (int t = 0; t < 5; ++t) {
          float mn = mo[t];
#pragma unroll
          for (int ww = 0; ww < 4; ++ww) { float r = *(LAS float*)(rb + ww * 320 + t * 64); asm volatile("" : "+v"(r)); mn = fmaxf(mn, ch[ww] ? r : NEG); }
          const float fo = ex2(mo[t] - mn);
          mo[t] = mn;
#pragma unroll
          for (int u = 0; u < 4; ++u) acc[u][t] *= fo;
          SCHED_FENCE();
        }
        BAR();
      }
#pragma unroll
      for (int ks = 0; ks < 2; ++ks)
#pragma unroll
        for (int t = 0; t < 5; ++t) pf[ks][t] = ld128(pr + (ks * 5 + t) * 1024);
      WAIT_LGKM0();
      if constexpr (MODE == 0) dma_rows<KV_NT>(vring + vs, ve, vo, srdV, (T + 2) * (64 * DH * 2), 8 * DH * 2);
      else if (v < 3) { const unsigned we = row_src(opaque(lane), DM * 2); dma_rows<W_NT>(vring + vs, we, we ^ 64, srdWo, (MODE - 1) * WOSLOT, WOP); }
      if constexpr (MODE == 2) BAR();
#pragma unroll
      for (int ks = 0; ks < 2; ++ks)
#pragma unroll
        for (int u = 0; u < 4; ++u)
#pragma unroll
          for (int t = 0; t < 5; ++t) acc[u][t] = mfma(af[ks][u], pf[ks][t], acc[u][t]);
      if constexpr (MODE != 2) BAR();
    };
    for (int T = 0; T < NTILE - 2; ++T) tile(T, IC<0>{});
    tile(NTILE - 2, IC<1>{});
    tile(NTILE - 1, IC<2>{});
    const int ln = opaque(lane);
#pragma unroll
    for (int t = 0; t < 5; ++t) {
      float ls = 0.f;
#pragma unroll
      for (int ww = 0; ww < 4; ++ww) ls += *(LAS float*)(smem + LDS_L + (ww * 80 + t * 16 + (ln & 15)) * 4);
      const float inv = 1.0f / ls;
#pragma unroll
      for (int u = 0; u < 4; ++u) *(LAS u32x2*)(xo + (u >> 1) * 5120 + (u & 1) * 512 + t * 1024) = pk4(acc[u][t] * inv);
    }
    WAIT_LGKM0(); BAR();
    if (v < 3) {
      bf16x8 wf[8][4];
      const unsigned we = row_src(ln, DM * 2);
      WAIT_VM(8);
      ld_slot(wf + 0, wring + VSLOT, ri);
      WAIT_LGKM0();
      dma_rows<W_NT>(vring + VSLOT, we, we ^ 64, srdWo, 2 * WOSLOT, WOP);
      WAIT_VM(8);
      ld_slot(wf + 2, wring, ri);
      WAIT_LGKM0();
      dma_rows<W_NT>(vring, we, we ^ 64, srdWo, 3 * WOSLOT, WOP);
      WAIT_VM(8);
      ld_slot(wf + 4, wring + VSLOT, ri);
      WAIT_VM(0);
      ld_slot(wf + 6, wring, ri);
      out_proj(wf, smem, pbase + ln * 16, ln);
    }
  }
}

__global__ __launch_bounds__(256)
void gqa_reduce(const char* __restrict__ part, char* __restrict__ out) {
  const int lane = threadIdx.x & 63, q = lane & 15, g = lane >> 4;
  const int id = blockIdx.x * 4 + (threadIdx.x >> 6);
  const int grp = id / NBLK, blk = id % NBLK;
  const char* src = part + ((size_t)grp * HQ * NBLK + blk) * 1024 + lane * 16;
  u32x4 v[16];
#pragma unroll
  for (int i = 0; i < 16; ++i) v[i] = *(const u32x4*)(src + (size_t)i * NBLK * 1024);
  float s[8];
#pragma unroll
  for (int k = 0; k < 8; ++k) s[k] = 0.f;
#pragma unroll
  for (int i = 0; i < 16; ++i)
#pragma unroll
    for (int k = 0; k < 4; ++k) { s[2 * k] += __uint_as_float(v[i][k] << 16); s[2 * k + 1] += __uint_as_float(v[i][k] & 0xffff0000u); }
  const int cb = blk / 10, t = (blk % 10) >> 1, m = blk & 1, br = grp >> 3, b = grp & 7;
  char* dst = out + ((size_t)(b * SQ + t * 16 + q) * (2 * DM) + br * DM + cb * 64 + m * 32 + g * 4) * 2;
  *(u32x2*)dst = u32x2{pk2(s[0], s[1]), pk2(s[2], s[3])};
  *(u32x2*)(dst + 32) = u32x2{pk2(s[4], s[5]), pk2(s[6], s[7])};
}

extern "C" void kernel_launch(void* const* d_in, const int* in_sizes, int n_in, void* d_out, int out_size, void* d_ws, size_t ws_size,
                              hipStream_t stream) {
  (void)in_sizes; (void)n_in; (void)out_size; (void)ws_size;
  gqa_main<<<256, 512, 0, stream>>>((const char*)d_in[0], (const char*)d_in[1], (const char*)d_in[2], (const char*)d_in[3], (const char*)d_in[4],
                                    (const char*)d_in[5], (const char*)d_in[6], (const char*)d_in[7], (const char*)d_in[8], (const char*)d_in[9],
                                    (char*)d_ws);
  gqa_reduce<<<16 * NBLK / 4, 256, 0, stream>>>((const char*)d_ws, (char*)d_out);
}
```

```cpp
#include <hip/hip_runtime.h>
#include <hip/hip_bf16.h>
#include <stdint.h>

#define KV_NT 1
#define W_NT 1

typedef __bf16 bf16x8 __attribute__((ext_vector_type(8)));
typedef __bf16 bf16x2 __attribute__((ext_vector_type(2)));
typedef short s16x4 __attribute__((ext_vector_type(4)));
typedef short s16x8 __attribute__((ext_vector_type(8)));
typedef float f32x4 __attribute__((ext_vector_type(4)));
typedef float f32x2 __attribute__((ext_vector_type(2)));
typedef unsigned u32x4 __attribute__((ext_vector_type(4)));
typedef unsigned u32x2 __attribute__((ext_vector_type(2)));
#define LAS __attribute__((address_space(3)))
typedef LAS char* lptr;

constexpr int NB = 8, SQ = 80, DM = 448, HQ = 16, HKV = 8, SK = 1024, DH = 256;
constexpr int NTILE = SK / 64;
constexpr int KPIECE = 1056, KSLOT = 8 * KPIECE, KRING = 2 * KSLOT;
constexpr int LDS_V = 4 * KRING, VSLOT = 8192, VRING = 16384;
constexpr int LDS_X = LDS_V + 3 * VRING;
constexpr int LDS_P = LDS_X + 20480, PBUF = 10240;
constexpr int LDS_REF = LDS_X + 40960;
constexpr int LDS_CHG = LDS_REF + 2560;
constexpr int LDS_L = LDS_CHG + 32;
constexpr int LDS_TOTAL = 163840;
static_assert(LDS_L + 1280 <= LDS_TOTAL, "lds");

constexpr float SCL = 0.0625f * 1.4426950408889634f;
constexpr float THR = 10.f;
constexpr float NEG = -1.0e30f;
constexpr int NBLK = 70;

#define WAIT_VM(n) asm volatile("s_waitcnt vmcnt(" #n ")" ::: "memory")
#define WAIT_LGKM0() asm volatile("s_waitcnt lgkmcnt(0)" ::: "memory")
#define BAR() do { __builtin_amdgcn_sched_barrier(0); asm volatile("" ::: "memory"); __builtin_amdgcn_s_barrier(); asm volatile("" ::: "memory"); __builtin_amdgcn_sched_barrier(0); } while (0)

#define STAMP(i) do {} while (0)
#define STAMP_DECL do {} while (0)
#define STAMP_FLUSH() do {} while (0)
#define SEG0() do {} while (0)
#define SEG(i) do {} while (0)

template <int NT>
__device__ __forceinline__ void dma16(unsigned m0v, unsigned voff, __amdgpu_buffer_rsrc_t srd, unsigned soff) {
  if (NT) asm volatile("s_mov_b32 m0, %0\n\ts_nop 0\n\tbuffer_load_dwordx4 %1, %2, %3 offen nt lds" :: "s"(m0v), "v"(voff), "s"(srd), "s"(soff) : "memory");
  else    asm volatile("s_mov_b32 m0, %0\n\ts_nop 0\n\tbuffer_load_dwordx4 %1, %2, %3 offen lds" :: "s"(m0v), "v"(voff), "s"(srd), "s"(soff) : "memory");
}
template <int NT>
__device__ __forceinline__ void dma_rows(unsigned lds, unsigned ve, unsigned vo, __amdgpu_buffer_rsrc_t srd, unsigned soff, unsigned pstride) {
#pragma unroll
  for (int p = 0; p < 8; ++p) dma16<NT>(lds + p * 1024, (p & 1) ? vo : ve, srd, soff + p * pstride);
}
template <int NT>
__device__ __forceinline__ void dma_k(unsigned lds, unsigned v, __amdgpu_buffer_rsrc_t srd, unsigned soff) {
#pragma unroll
  for (int p = 0; p < 8; ++p) dma16<NT>(lds + p * KPIECE, v, srd, soff + p * 1024);
}
__device__ __forceinline__ bf16x8 ld128(lptr p) { return *(LAS bf16x8*)p; }
__device__ __forceinline__ bf16x8 ldtr(lptr p) {
  s16x4 a = __builtin_amdgcn_ds_read_tr16_b64_v4i16((LAS s16x4*)p);
  s16x4 b = __builtin_amdgcn_ds_read_tr16_b64_v4i16((LAS s16x4*)(p + 512));
  return __builtin_bit_cast(bf16x8, __builtin_shufflevector(a, b, 0, 1, 2, 3, 4, 5, 6, 7));
}
__device__ __forceinline__ unsigned pk2(float a, float b) { f32x2 t = {a, b}; return __builtin_bit_cast(unsigned, __builtin_convertvector(t, bf16x2)); }
__device__ __forceinline__ u32x2 pk4(f32x4 v) { u32x2 r = {pk2(v[0], v[1]), pk2(v[2], v[3])}; return r; }
__device__ __forceinline__ f32x4 mfma(bf16x8 a, bf16x8 b, f32x4 c) { return __builtin_amdgcn_mfma_f32_16x16x32_bf16(a, b, c, 0, 0, 0); }
__device__ __forceinline__ float ex2(float x) { return __builtin_amdgcn_exp2f(x); }
__device__ __forceinline__ float red_max_g(float v) {
  auto a = __builtin_amdgcn_permlane16_swap(__float_as_uint(v), __float_as_uint(v), false, false);
  v = fmaxf(__uint_as_float(a[0]), __uint_as_float(a[1]));
  auto b = __builtin_amdgcn_permlane32_swap(__float_as_uint(v), __float_as_uint(v), false, false);
  return fmaxf(__uint_as_float(b[0]), __uint_as_float(b[1]));
}
__device__ __forceinline__ float red_sum_g(float v) {
  auto a = __builtin_amdgcn_permlane16_swap(__float_as_uint(v), __float_as_uint(v), false, false);
  v = __uint_as_float(a[0]) + __uint_as_float(a[1]);
  auto b = __builtin_amdgcn_permlane32_swap(__float_as_uint(v), __float_as_uint(v), false, false);
  return __uint_as_float(b[0]) + __uint_as_float(b[1]);
}

template <int V> struct IC { static constexpr int value = V; };
__device__ __forceinline__ int opaque(int v) { asm volatile("" : "+v"(v)); return v; }
#define SCHED_FENCE() __builtin_amdgcn_sched_barrier(0)

struct RowImg { int trb, fsw; };
__device__ __forceinline__ RowImg row_img(int lane) {
  RowImg r; r.fsw = ((lane >> 3) & 1) | (((lane >> 4) & 1) << 1);
  r.trb = (8 * (lane >> 4) + ((lane >> 2) & 3)) * 128 + 8 * (lane & 3); return r;
}
__device__ __forceinline__ unsigned row_src(int lane, int stride) {
  const int srow = lane >> 3; return srow * stride + (((lane & 7) ^ (((srow >> 1) & 1) << 1)) * 16);
}
__device__ __forceinline__ void ld_slot(bf16x8 (*dst)[4], lptr base, const RowImg& ri) {
#pragma unroll
  for (int ks = 0; ks < 2; ++ks)
#pragma unroll
    for (int u = 0; u < 4; ++u) dst[ks][u] = ldtr(base + ri.trb + 32 * (u ^ ri.fsw) + 4096 * ks);
}
__device__ __forceinline__ void out_proj(bf16x8 (*wf)[4], lptr smem, char* pdst, int lane) {
#pragma unroll
  for (int t = 0; t < 5; ++t) {
    bf16x8 of[8];
#pragma unroll
    for (int ks = 0; ks < 8; ++ks) of[ks] = ld128(smem + LDS_X + (3 - (ks >> 1)) * PBUF + ((ks & 1) * 5 + t) * 1024 + lane * 16);
    f32x4 o[4];
#pragma unroll
    for (int u = 0; u < 4; ++u) o[u] = f32x4{0.f, 0.f, 0.f, 0.f};
#pragma unroll
    for (int ks = 0; ks < 8; ++ks)
#pragma unroll
      for (int u = 0; u < 4; ++u) o[u] = mfma(wf[ks][u], of[ks], o[u]);
#pragma unroll
    for (int m = 0; m < 2; ++m) {
      const u32x2 a = pk4(o[2 * m]), c = pk4(o[2 * m + 1]);
      *(u32x4*)(pdst + (t * 2 + m) * 1024) = u32x4{a[0], a[1], c[0], c[1]};
    }
  }
}

__global__ __launch_bounds__(512, 2)
void gqa_main(const char* __restrict__ xA, const char* __restrict__ kA, const char* __restrict__ vA, const char* __restrict__ wqA, const char* __restrict__ woA,
              const char* __restrict__ xB, const char* __restrict__ kB, const char* __restrict__ vB, const char* __restrict__ wqB, const char* __restrict__ woB,
              char* __restrict__ part) {
  __shared__ __attribute__((aligned(16))) char smem_raw[LDS_TOTAL];
  const lptr smem = (lptr)smem_raw;
  const unsigned ldsb = (unsigned)(uintptr_t)smem;
  const int tid = threadIdx.x;
  const int wid = __builtin_amdgcn_readfirstlane(tid >> 6);
  const int bid = blockIdx.x, xcd = bid & 7, jj = bid >> 3;
  const int br = xcd >> 2, b = jj >> 2, kvh = 2 * (xcd & 3) + ((jj >> 1) & 1), h = 2 * kvh + (jj & 1);
  const __amdgpu_buffer_rsrc_t srdWo = __builtin_amdgcn_make_buffer_rsrc((void*)((br ? woB : woA) + (size_t)h * DH * DM * 2 + wid * 128), (short)0, DH * DM * 2 - wid * 128, 0x00020000);
  char* const pbase = part + ((size_t)((br * NB + b) * HQ + h) * NBLK + wid * 10) * 1024;
  constexpr unsigned WOSLOT = 64 * DM * 2, WOP = 8 * DM * 2;
  STAMP_DECL;
  STAMP(0);

  if (wid < 4) {
    const int w = wid;
    __builtin_amdgcn_s_setprio(1);
    const char* x = (br ? xB : xA) + (size_t)b * SQ * DM * 2;
    const __amdgpu_buffer_rsrc_t srdK = __builtin_amdgcn_make_buffer_rsrc((void*)((br ? kB : kA) + ((size_t)(b * HKV + kvh) * SK + w * 16) * (DH * 2)), (short)0, (SK - w * 16) * DH * 2, 0x00020000);
    const unsigned kring = ldsb + w * KRING;
    const int lane = tid & 63;
    const unsigned kv = (lane >> 5) * 512 + (((lane & 31) ^ (lane >> 5)) * 16);
    {
      u32x4 xr[7][3];
      unsigned xo[3];
#pragma unroll
      for (int i = 0; i < 3; ++i) { int blk = w + 4 * i; blk = blk < 10 ? blk : 9; xo[i] = ((blk % 5) * 16 + (lane & 15)) * (DM * 2) + (blk / 5) * 64 + (lane >> 4) * 16; }
#pragma unroll
      for (int j = 0; j < 7; ++j)
#pragma unroll
        for (int i = 0; i < 3; ++i)
          asm volatile("global_load_dwordx4 %0, %1, %2 offset:%3" : "=v"(xr[j][i]) : "v"(xo[i]), "s"(x), "i"(j * 128) : "memory");
      dma_k<KV_NT>(kring, kv, srdK, 0);
      dma_k<KV_NT>(kring + KSLOT, kv, srdK, 64 * DH * 2);
      if (lane < 8) *(LAS unsigned*)(smem + LDS_CHG + lane * 4) = 0;
      asm volatile("s_waitcnt vmcnt(16)"
                   : "+v"(xr[0][0]), "+v"(xr[0][1]), "+v"(xr[0][2]), "+v"(xr[1][0]), "+v"(xr[1][1]), "+v"(xr[1][2]), "+v"(xr[2][0]), "+v"(xr[2][1]), "+v"(xr[2][2]),
                     "+v"(xr[3][0]), "+v"(xr[3][1]), "+v"(xr[3][2]), "+v"(xr[4][0]), "+v"(xr[4][1]), "+v"(xr[4][2]), "+v"(xr[5][0]), "+v"(xr[5][1]), "+v"(xr[5][2]),
                     "+v"(xr[6][0]), "+v"(xr[6][1]), "+v"(xr[6][2]) :: "memory");
      const lptr xdst = smem + LDS_P + w * 1024 + lane * 16;
#define XWRITE(j) do { *(LAS u32x4*)(xdst + ((j) & 1) * PBUF) = xr[j][0]; *(LAS u32x4*)(xdst + ((j) & 1) * PBUF + 4096) = xr[j][1]; \
                       if (w < 2) *(LAS u32x4*)(xdst + ((j) & 1) * PBUF + 8192) = xr[j][2]; } while (0)
      XWRITE(0); XWRITE(1);
      STAMP(1);
      WAIT_LGKM0(); BAR();
      STAMP(2);
#pragma unroll
      for (int j = 0; j < 7; ++j) {
        BAR();
        if (j + 2 <= 6) { XWRITE(j + 2 <= 6 ? j + 2 : 0); WAIT_LGKM0(); }
      }
#undef XWRITE
    }
    BAR();
    STAMP(3);
    bf16x8 qf[8][5];
#pragma unroll
    for (int ks = 0; ks < 8; ++ks)
#pragma unroll
      for (int t = 0; t < 5; ++t) qf[ks][t] = ld128(smem + LDS_X + (3 - (ks >> 1)) * PBUF + ((ks & 1) * 5 + t) * 1024 + lane * 16);
    WAIT_LGKM0(); BAR();
    STAMP(4);
    float ref[5], l[5];
#pragma unroll
    for (int t = 0; t < 5; ++t) { ref[t] = NEG; l[t] = 0.f; }
    const lptr ka0 = smem + w * KRING + ((lane & 15) >> 1) * KPIECE + (lane & 1) * 512 + (((lane >> 4) ^ (lane & 1)) * 16);
    const lptr pw0 = smem + LDS_P + (w >> 1) * 5120 + (w & 1) * 512 + 256 * (lane >> 5) + 16 * (lane & 15) + 8 * ((lane >> 4) & 1);
    auto adopt = [&](int par) {
      const u32x4 ch = *(LAS u32x4*)(smem + LDS_CHG + par * 16);
      if (__builtin_amdgcn_readfirstlane(ch[0] | ch[1] | ch[2] | ch[3])) {
        const lptr rb = smem + LDS_REF + par * 1280 + (opaque(lane) & 15) * 4;
#pragma unroll
        for (int t = 0; t < 5; ++t) {
          float m = ref[t];
#pragma unroll
          for (int ww = 0; ww < 4; ++ww) { float r = *(LAS float*)(rb + ww * 320 + t * 64); asm volatile("" : "+v"(r)); m = fmaxf(m, ch[ww] ? r : NEG); }
          const float f = ex2(ref[t] - m);
          l[t] *= f; ref[t] = m;
          LAS u32x2* pp = (LAS u32x2*)(pw0 + par * PBUF + t * 1024);
          const u32x2 pv = *pp;
          *pp = u32x2{pk2(__uint_as_float(pv[0] << 16) * f, __uint_as_float(pv[0] & 0xffff0000u) * f), pk2(__uint_as_float(pv[1] << 16) * f, __uint_as_float(pv[1] & 0xffff0000u) * f)};
          SCHED_FENCE();
        }
        WAIT_LGKM0(); BAR();
      }
    };
    auto tile = [&](int T, auto mode) {
      SEG0();
      WAIT_VM(8);
      SEG(0);
      const lptr ka = ka0 + (T & 1) * KSLOT;
      bf16x8 kf[8];
#pragma unroll
      for (int i = 0; i < 8; ++i) kf[i] = ld128(ka + 64 * i);
      adopt((T + 1) & 1);
      WAIT_LGKM0();
      if constexpr (decltype(mode)::value == 0) dma_k<KV_NT>(kring + (T & 1) * KSLOT, kv, srdK, (T + 2) * (64 * DH * 2));
      else { const unsigned we = row_src(opaque(lane), DM * 2); dma_rows<W_NT>(kring + (decltype(mode)::value - 1) * KSLOT, we, we ^ 64, srdWo, (decltype(mode)::value - 1) * WOSLOT, WOP); }
      SEG(1);
      f32x4 s[5];
#pragma unroll
      for (int t = 0; t < 5; ++t) s[t] = f32x4{0.f, 0.f, 0.f, 0.f};
#pragma unroll
      for (int i = 0; i < 8; ++i)
#pragma unroll
        for (int t = 0; t < 5; ++t) s[t] = mfma(kf[i], qf[i][t], s[t]);
      float mx = NEG;
#pragma unroll
      for (int t = 0; t < 5; ++t) mx = fmaxf(mx, fmaf(fmaxf(fmaxf(s[t][0], s[t][1]), fmaxf(s[t][2], s[t][3])), SCL, -ref[t]));
      unsigned changed = 0;
      if (__builtin_amdgcn_ballot_w64(mx > THR)) {
        changed = 1;
        const int ln = opaque(lane);
        const lptr rb = smem + LDS_REF + ((T & 1) * 4 + w) * 320 + (ln & 15) * 4;
#pragma unroll
        for (int t = 0; t < 5; ++t) {
          const float m = red_max_g(fmaxf(fmaxf(s[t][0], s[t][1]), fmaxf(s[t][2], s[t][3])));
          const float nr = fmaxf(ref[t], ceilf(m * SCL));
          l[t] *= ex2(ref[t] - nr); ref[t] = nr;
          if (ln < 16) *(LAS float*)(rb + t * 64) = nr;
          SCHED_FENCE();
        }
      }
      const lptr pw = pw0 + (T & 1) * PBUF;
#pragma unroll
      for (int t = 0; t < 5; ++t) {
        f32x4 p;
#pragma unroll
        for (int r = 0; r < 4; ++r) p[r] = ex2(fmaf(s[t][r], SCL, -ref[t]));
        l[t] += (p[0] + p[1]) + (p[2] + p[3]);
        *(LAS u32x2*)(pw + t * 1024) = pk4(p);
      }
      if (lane == 0) *(LAS unsigned*)(smem + LDS_CHG + ((T & 1) * 4 + w) * 4) = changed;
      WAIT_LGKM0();
      SEG(2);
      BAR();
      SEG(3);
    };
    for (int T = 0; T < NTILE - 2; ++T) { tile(T, IC<0>{}); if (T == 1) STAMP(5); if (T == 7) STAMP(6); }
    tile(NTILE - 2, IC<1>{});
    tile(NTILE - 1, IC<2>{});
    STAMP(7);
    adopt(1);
    const int ln = opaque(lane);
#pragma unroll
    for (int t = 0; t < 5; ++t) { const float ls = red_sum_g(l[t]); if (ln < 16) *(LAS float*)(smem + LDS_L + (w * 80 + t * 16 + ln) * 4) = ls; }
    WAIT_LGKM0(); BAR();
    bf16x8 wf[8][4];
    const lptr wring = smem + w * KRING;
    const RowImg ri = row_img(ln);
    const unsigned we = row_src(ln, DM * 2);
    WAIT_VM(8);
    ld_slot(wf + 0, wring, ri);
    WAIT_LGKM0();
    dma_rows<W_NT>(kring, we, we ^ 64, srdWo, 2 * WOSLOT, WOP);
    WAIT_VM(8);
    ld_slot(wf + 2, wring + KSLOT, ri);
    WAIT_LGKM0();
    dma_rows<W_NT>(kring + KSLOT, we, we ^ 64, srdWo, 3 * WOSLOT, WOP);
    BAR();
    STAMP(8);
    WAIT_VM(8);
    ld_slot(wf + 4, wring, ri);
    WAIT_VM(0);
    ld_slot(wf + 6, wring + KSLOT, ri);
    STAMP(9);
    out_proj(wf, smem, pbase + ln * 16, ln);
    STAMP(10);
    WAIT_VM(0);
    STAMP(11);
  } else {
    const int v = wid - 4;
    const int lane = tid & 63;
    const __amdgpu_buffer_rsrc_t srdQ = __builtin_amdgcn_make_buffer_rsrc((void*)((br ? wqB : wqA) + (h * DH + v * 64) * 2), (short)0, DM * HQ * DH * 2 - (h * DH + v * 64) * 2, 0x00020000);
    const __amdgpu_buffer_rsrc_t srdV = __builtin_amdgcn_make_buffer_rsrc((void*)((br ? vB : vA) + (size_t)(b * HKV + kvh) * SK * DH * 2 + v * 128), (short)0, SK * DH * 2 - v * 128, 0x00020000);
    const unsigned vring = ldsb + LDS_V + v * VRING;
    const unsigned wqe = row_src(lane, HQ * DH * 2), ve = row_src(lane, DH * 2), vo = ve ^ 64;
    dma_rows<W_NT>(vring, wqe, wqe ^ 64, srdQ, 0, 8 * HQ * DH * 2);
    dma_rows<W_NT>(vring + VSLOT, wqe, wqe ^ 64, srdQ, 64 * HQ * DH * 2, 8 * HQ * DH * 2);
    const RowImg ri = row_img(lane);
    const lptr wring = smem + LDS_V + v * VRING;
    const lptr pr0 = smem + LDS_P + lane * 16;
    f32x4 acc[4][5];
#pragma unroll
    for (int u = 0; u < 4; ++u)
#pragma unroll
      for (int t = 0; t < 5; ++t) acc[u][t] = f32x4{0.f, 0.f, 0.f, 0.f};
    BAR();
    STAMP(2);
    {
      bf16x8 pf[2][2][5];
#pragma unroll
      for (int ks = 0; ks < 2; ++ks)
#pragma unroll
        for (int t = 0; t < 5; ++t) pf[0][ks][t] = ld128(pr0 + (ks * 5 + t) * 1024);
#pragma unroll
      for (int j = 0; j < 7; ++j) {
        WAIT_LGKM0(); BAR();
        if (j == 6 && v == 3) WAIT_VM(0); else WAIT_VM(8);
        bf16x8 af[2][4];
        ld_slot(af, wring + (j & 1) * VSLOT, ri);
        WAIT_LGKM0();
        if (j + 2 <= 6) dma_rows<W_NT>(vring + (j & 1) * VSLOT, wqe, wqe ^ 64, srdQ, (j + 2) * (64 * HQ * DH * 2), 8 * HQ * DH * 2);
        else if (v < 3) dma_rows<KV_NT>(vring + (j & 1) * VSLOT, ve, vo, srdV, (j - 5) * (64 * DH * 2), 8 * DH * 2);
        if (j < 6) {
#pragma unroll
          for (int ks = 0; ks < 2; ++ks)
#pragma unroll
            for (int t = 0; t < 5; ++t) pf[(j + 1) & 1][ks][t] = ld128(pr0 + ((j + 1) & 1) * PBUF + (ks * 5 + t) * 1024);
        }
#pragma unroll
        for (int ks = 0; ks < 2; ++ks)
#pragma unroll
          for (int u = 0; u < 4; ++u)
#pragma unroll
            for (int t = 0; t < 5; ++t) acc[u][t] = mfma(af[ks][u], pf[j & 1][ks][t], acc[u][t]);
      }
    }
    const lptr xo = smem + LDS_X + (3 - v) * PBUF + 256 * (lane >> 5) + 16 * (lane & 15) + 8 * ((lane >> 4) & 1);
#pragma unroll
    for (int u = 0; u < 4; ++u)
#pragma unroll
      for (int t = 0; t < 5; ++t) { *(LAS u32x2*)(xo + (u >> 1) * 5120 + (u & 1) * 512 + t * 1024) = pk4(acc[u][t]); acc[u][t] = f32x4{0.f, 0.f, 0.f, 0.f}; }
    STAMP(3);
    WAIT_LGKM0(); BAR();
    BAR();
    STAMP(4);
    if (v == 3) {
      dma_rows<KV_NT>(vring + VSLOT, ve, vo, srdV, 0, 8 * DH * 2);
      dma_rows<KV_NT>(vring, ve, vo, srdV, 64 * DH * 2, 8 * DH * 2);
    }
    float mo[5];
#pragma unroll
    for (int t = 0; t < 5; ++t) mo[t] = NEG;
    BAR();
    auto tile = [&](int T, auto mode) {
      constexpr int MODE = decltype(mode)::value;
      SEG0();
      if (MODE == 2 && v == 3) WAIT_VM(0); else WAIT_VM(8);
      SEG(0);
      const int vs = ((T + 1) & 1) * VSLOT;
      const lptr pr = pr0 + (T & 1) * PBUF;
      bf16x8 af[2][4], pf[2][5];
      const u32x4 ch = *(LAS u32x4*)(smem + LDS_CHG + (T & 1) * 16);
      ld_slot(af, wring + vs, ri);
      if (__builtin_amdgcn_readfirstlane(ch[0] | ch[1] | ch[2] | ch[3])) {
        const lptr rb = smem + LDS_REF + (T & 1) * 1280 + (opaque(lane) & 15) * 4;
#pragma unroll
        for (int t = 0; t < 5; ++t) {
          float mn = mo[t];
#pragma unroll
          for (int ww = 0; ww < 4; ++ww) { float r = *(LAS float*)(rb + ww * 320 + t * 64); asm volatile("" : "+v"(r)); mn = fmaxf(mn, ch[ww] ? r : NEG); }
          const float fo = ex2(mo[t] - mn);
          mo[t] = mn;
#pragma unroll
          for (int u = 0; u < 4; ++u) acc[u][t] *= fo;
          SCHED_FENCE();
        }
        BAR();
      }
#pragma unroll
      for (int ks = 0; ks < 2; ++ks)
#pragma unroll
        for (int t = 0; t < 5; ++t) pf[ks][t] = ld128(pr + (ks * 5 + t) * 1024);
      WAIT_LGKM0();
      if constexpr (MODE == 0) dma_rows<KV_NT>(vring + vs, ve, vo, srdV, (T + 2) * (64 * DH * 2), 8 * DH * 2);
      else if (v < 3) { const unsigned we = row_src(opaque(lane), DM * 2); dma_rows<W_NT>(vring + vs, we, we ^ 64, srdWo, (MODE - 1) * WOSLOT, WOP); }
      SEG(1);
      if constexpr (MODE == 2) BAR();
#pragma unroll
      for (int ks = 0; ks < 2; ++ks)
#pragma unroll
        for (int u = 0; u < 4; ++u)
#pragma unroll
          for (int t = 0; t < 5; ++t) acc[u][t] = mfma(af[ks][u], pf[ks][t], acc[u][t]);
      SEG(2);
      if constexpr (MODE != 2) BAR();
      SEG(3);
    };
    for (int T = 0; T < NTILE - 2; ++T) { tile(T, IC<0>{}); if (T == 0) STAMP(5); if (T == 6) STAMP(6); }
    tile(NTILE - 2, IC<1>{});
    STAMP(7);
    tile(NTILE - 1, IC<2>{});
    const int ln = opaque(lane);
#pragma unroll
    for (int t = 0; t < 5; ++t) {
      float ls = 0.f;
#pragma unroll
      for (int ww = 0; ww < 4; ++ww) ls += *(LAS float*)(smem + LDS_L + (ww * 80 + t * 16 + (ln & 15)) * 4);
      const float inv = 1.0f / ls;
#pragma unroll
      for (int u = 0; u < 4; ++u) *(LAS u32x2*)(xo + (u >> 1) * 5120 + (u & 1) * 512 + t * 1024) = pk4(acc[u][t] * inv);
    }
    WAIT_LGKM0(); BAR();
    STAMP(8);
    if (v < 3) {
      bf16x8 wf[8][4];
      const unsigned we = row_src(ln, DM * 2);
      WAIT_VM(8);
      ld_slot(wf + 0, wring + VSLOT, ri);
      WAIT_LGKM0();
      dma_rows<W_NT>(vring + VSLOT, we, we ^ 64, srdWo, 2 * WOSLOT, WOP);
      WAIT_VM(8);
      ld_slot(wf + 2, wring, ri);
      WAIT_LGKM0();
      dma_rows<W_NT>(vring, we, we ^ 64, srdWo, 3 * WOSLOT, WOP);
      WAIT_VM(8);
      ld_slot(wf + 4, wring + VSLOT, ri);
      WAIT_VM(0);
      ld_slot(wf + 6, wring, ri);
      STAMP(9);
      out_proj(wf, smem, pbase + ln * 16, ln);
      STAMP(10);
      WAIT_VM(0);
      STAMP(11);
    }
  }
  STAMP_FLUSH();
}

__global__ __launch_bounds__(256)
void gqa_reduce(const char* __restrict__ part, char* __restrict__ out) {
  const int lane = threadIdx.x & 63, q = lane & 15, g = lane >> 4;
  const int id = blockIdx.x * 4 + (threadIdx.x >> 6);
  const int grp = id / NBLK, blk = id % NBLK;
  const char* src = part + ((size_t)grp * HQ * NBLK + blk) * 1024 + lane * 16;
  u32x4 v[16];
#pragma unroll
  for (int i = 0; i < 16; ++i) v[i] = *(const u32x4*)(src + (size_t)i * NBLK * 1024);
  float s[8];
#pragma unroll
  for (int k = 0; k < 8; ++k) s[k] = 0.f;
#pragma unroll
  for (int i = 0; i < 16; ++i)
#pragma unroll
    for (int k = 0; k < 4; ++k) { s[2 * k] += __uint_as_float(v[i][k] << 16); s[2 * k + 1] += __uint_as_float(v[i][k] & 0xffff0000u); }
  const int cb = blk / 10, t = (blk % 10) >> 1, m = blk & 1, br = grp >> 3, b = grp & 7;
  char* dst = out + ((size_t)(b * SQ + t * 16 + q) * (2 * DM) + br * DM + cb * 64 + m * 32 + g * 4) * 2;
  *(u32x2*)dst = u32x2{pk2(s[0], s[1]), pk2(s[2], s[3])};
  *(u32x2*)(dst + 32) = u32x2{pk2(s[4], s[5]), pk2(s[6], s[7])};
}

extern "C" void kernel_launch(void* const* d_in, const int* in_sizes, int n_in, void* d_out, int out_size, void* d_ws, size_t ws_size,
                              hipStream_t stream) {
  (void)in_sizes; (void)n_in; (void)out_size; (void)ws_size;
  gqa_main<<<256, 512, 0, stream>>>((const char*)d_in[0], (const char*)d_in[1], (const char*)d_in[2], (const char*)d_in[3], (const char*)d_in[4],
                                    (const char*)d_in[5], (const char*)d_in[6], (const char*)d_in[7], (const char*)d_in[8], (const char*)d_in[9],
                                    (char*)d_ws);
  gqa_reduce<<<16 * NBLK / 4, 256, 0, stream>>>((const char*)d_ws, (char*)d_out);
}
```

```cpp
#include <hip/hip_runtime.h>
#include <hip/hip_bf16.h>
#include <stdint.h>

#define KV_NT 1
#define W_NT 0
#define FUSED 1

typedef __bf16 bf16x8 __attribute__((ext_vector_type(8)));
typedef __bf16 bf16x2 __attribute__((ext_vector_type(2)));
typedef short s16x4 __attribute__((ext_vector_type(4)));
typedef float f32x4 __attribute__((ext_vector_type(4)));
typedef float f32x2 __attribute__((ext_vector_type(2)));
typedef unsigned u32x4 __attribute__((ext_vector_type(4)));
typedef unsigned u32x2 __attribute__((ext_vector_type(2)));
#define LAS __attribute__((address_space(3)))
#define GAS __attribute__((address_space(1)))
typedef LAS char* lptr;
typedef GAS unsigned long long gu64;
typedef __amdgpu_buffer_rsrc_t rsrc_t;

constexpr int NB = 8, SQ = 80, DM = 448, HQ = 16, HKV = 8, SK = 1024, DH = 256;
constexpr int NTILE = SK / 64;
constexpr int KPIECE = 1056, KSLOT = 8 * KPIECE, KRING = 2 * KSLOT;
constexpr int LDS_V = 4 * KRING, VSLOT = 8192, VRING = 16384;
constexpr int LDS_X = LDS_V + 3 * VRING;
constexpr int LDS_P = LDS_X + 20480, PBUF = 10240;
constexpr int LDS_REF = LDS_X + 40960;
constexpr int LDS_CHG = LDS_REF + 2560;
constexpr int LDS_L = LDS_CHG + 32;
constexpr int LDS_SCR = LDS_L + 1280;
constexpr int LDS_TOTAL = 163840;
static_assert(LDS_SCR + 256 <= LDS_TOTAL, "lds");

constexpr float SCL = 0.0625f * 1.4426950408889634f;
constexpr float THR = 10.f;
constexpr float NEG = -1.0e30f;
constexpr int NBLK = 70;
constexpr size_t WS_PART = 1 << 20;
constexpr unsigned OOB = 0x7ffffff0u;

#define WAIT_VM(n) asm volatile("s_waitcnt vmcnt(" #n ")" ::: "memory")
#define WAIT_LGKM0() asm volatile("s_waitcnt lgkmcnt(0)" ::: "memory")
#define SCHED_FENCE() __builtin_amdgcn_sched_barrier(0)
#define BAR() do { SCHED_FENCE(); asm volatile("" ::: "memory"); __builtin_amdgcn_s_barrier(); asm volatile("" ::: "memory"); SCHED_FENCE(); } while (0)

#define STAMP(i) do {} while (0)
#define STAMP_DECL do {} while (0)
#define STAMP_FLUSH() do {} while (0)
#define SEG0() do {} while (0)
#define SEG(i) do {} while (0)

template <int NT>
__device__ __forceinline__ void dma16(unsigned m0v, unsigned voff, rsrc_t srd, unsigned soff) {
  if (NT) asm volatile("s_mov_b32 m0, %0\n\ts_nop 0\n\tbuffer_load_dwordx4 %1, %2, %3 offen nt lds" :: "s"(m0v), "v"(voff), "s"(srd), "s"(soff) : "memory");
  else    asm volatile("s_mov_b32 m0, %0\n\ts_nop 0\n\tbuffer_load_dwordx4 %1, %2, %3 offen lds" :: "s"(m0v), "v"(voff), "s"(srd), "s"(soff) : "memory");
}
__device__ __forceinline__ void touch(unsigned m0v, unsigned voff, rsrc_t srd) {
  asm volatile("s_mov_b32 m0, %0\n\ts_nop 0\n\tbuffer_load_dword %1, %2, 0 offen lds" :: "s"(m0v), "v"(voff), "s"(srd) : "memory");
}
template <int NT>
__device__ __forceinline__ void dma_row_piece(int p, unsigned lds, unsigned ve, unsigned vo, rsrc_t srd, unsigned soff, unsigned pstride) {
  dma16<NT>(lds + p * 1024, (p & 1) ? vo : ve, srd, soff + p * pstride);
}
template <int NT>
__device__ __forceinline__ void dma_rows(unsigned lds, unsigned ve, unsigned vo, rsrc_t srd, unsigned soff, unsigned pstride) {
#pragma unroll
  for (int p = 0; p < 8; ++p) dma_row_piece<NT>(p, lds, ve, vo, srd, soff, pstride);
}
__device__ __forceinline__ bf16x8 ld128(lptr p) { return *(LAS bf16x8*)p; }
__device__ __forceinline__ bf16x8 ldtr(lptr p) {
  s16x4 a = __builtin_amdgcn_ds_read_tr16_b64_v4i16((LAS s16x4*)p);
  s16x4 b = __builtin_amdgcn_ds_read_tr16_b64_v4i16((LAS s16x4*)(p + 512));
  return __builtin_bit_cast(bf16x8, __builtin_shufflevector(a, b, 0, 1, 2, 3, 4, 5, 6, 7));
}
__device__ __forceinline__ unsigned pk2(float a, float b) { f32x2 t = {a, b}; return __builtin_bit_cast(unsigned, __builtin_convertvector(t, bf16x2)); }
__device__ __forceinline__ u32x2 pk4(f32x4 v) { u32x2 r = {pk2(v[0], v[1]), pk2(v[2], v[3])}; return r; }
__device__ __forceinline__ float blo(unsigned u) { return __uint_as_float(u << 16); }
__device__ __forceinline__ float bhi(unsigned u) { return __uint_as_float(u & 0xffff0000u); }
__device__ __forceinline__ f32x4 mfma(bf16x8 a, bf16x8 b, f32x4 c) { return __builtin_amdgcn_mfma_f32_16x16x32_bf16(a, b, c, 0, 0, 0); }
__device__ __forceinline__ float ex2(float x) { return __builtin_amdgcn_exp2f(x); }
__device__ __forceinline__ float red_max_g(float v) {
  auto a = __builtin_amdgcn_permlane16_swap(__float_as_uint(v), __float_as_uint(v), false, false);
  v = fmaxf(__uint_as_float(a[0]), __uint_as_float(a[1]));
  auto b = __builtin_amdgcn_permlane32_swap(__float_as_uint(v), __float_as_uint(v), false, false);
  return fmaxf(__uint_as_float(b[0]), __uint_as_float(b[1]));
}
__device__ __forceinline__ float red_sum_g(float v) {
  auto a = __builtin_amdgcn_permlane16_swap(__float_as_uint(v), __float_as_uint(v), false, false);
  v = __uint_as_float(a[0]) + __uint_as_float(a[1]);
  auto b = __builtin_amdgcn_permlane32_swap(__float_as_uint(v), __float_as_uint(v), false, false);
  return __uint_as_float(b[0]) + __uint_as_float(b[1]);
}
template <int V> struct IC { static constexpr int value = V; };
__device__ __forceinline__ int opaque(int v) { asm volatile("" : "+v"(v)); return v; }
__device__ __forceinline__ rsrc_t mk_srd(const void* p, unsigned bytes) { return __builtin_amdgcn_make_buffer_rsrc((void*)p, (short)0, (int)bytes, 0x00020000); }

struct RowImg { int trb, fsw; };
__device__ __forceinline__ RowImg row_img(int lane) {
  RowImg r; r.fsw = ((lane >> 3) & 1) | (((lane >> 4) & 1) << 1);
  r.trb = (8 * (lane >> 4) + ((lane >> 2) & 3)) * 128 + 8 * (lane & 3); return r;
}
__device__ __forceinline__ unsigned row_src(int lane, int stride) {
  const int srow = lane >> 3; return srow * stride + (((lane & 7) ^ (((srow >> 1) & 1) << 1)) * 16);
}
__device__ __forceinline__ void ld_slot(bf16x8 (*dst)[4], lptr base, const RowImg& ri) {
#pragma unroll
  for (int ks = 0; ks < 2; ++ks)
#pragma unroll
    for (int u = 0; u < 4; ++u) dst[ks][u] = ldtr(base + ri.trb + 32 * (u ^ ri.fsw) + 4096 * ks);
}
__device__ __forceinline__ void out_proj(bf16x8 (*wf)[4], lptr smem, rsrc_t srdP, int lane) {
  bf16x8 of[2][8];
  const lptr ob = smem + LDS_X + lane * 16;
#pragma unroll
  for (int ks = 0; ks < 8; ++ks) of[0][ks] = ld128(ob + (3 - (ks >> 1)) * PBUF + (ks & 1) * 5120);
#pragma unroll
  for (int t = 0; t < 5; ++t) {
    if (t < 4) {
#pragma unroll
      for (int ks = 0; ks < 8; ++ks) of[(t + 1) & 1][ks] = ld128(ob + (3 - (ks >> 1)) * PBUF + (ks & 1) * 5120 + (t + 1) * 1024);
    }
    f32x4 o[4];
#pragma unroll
    for (int u = 0; u < 4; ++u) o[u] = f32x4{0.f, 0.f, 0.f, 0.f};
#pragma unroll
    for (int ks = 0; ks < 8; ++ks)
#pragma unroll
      for (int u = 0; u < 4; ++u) o[u] = mfma(wf[ks][u], of[t & 1][ks], o[u]);
#pragma unroll
    for (int m = 0; m < 2; ++m) {
      const u32x2 a = pk4(o[2 * m]), c = pk4(o[2 * m + 1]);
      __builtin_amdgcn_raw_buffer_store_b128(u32x4{a[0], a[1], c[0], c[1]}, srdP, lane * 16 + (t * 2 + m) * 1024, 0, FUSED ? 16 : 0);
    }
    SCHED_FENCE();
  }
}
template <int AUX>
__device__ __forceinline__ void reduce_block(const char* ws, char* out, int grp, int blk, int lane) {
  const rsrc_t srd = mk_srd(ws + WS_PART + ((size_t)grp * HQ * NBLK + blk) * 1024, (15 * NBLK + 1) * 1024);
  u32x4 v[16];
#pragma unroll
  for (int i = 0; i < 16; ++i) v[i] = __builtin_amdgcn_raw_buffer_load_b128(srd, lane * 16, i * NBLK * 1024, AUX);
  float s[8];
#pragma unroll
  for (int k = 0; k < 8; ++k) s[k] = 0.f;
#pragma unroll
  for (int i = 0; i < 16; ++i)
#pragma unroll
    for (int k = 0; k < 4; ++k) { s[2 * k] += blo(v[i][k]); s[2 * k + 1] += bhi(v[i][k]); }
  const int cb = blk / 10, t = (blk % 10) >> 1, m = blk & 1, br = grp >> 3, b = grp & 7;
  char* dst = out + ((size_t)(b * SQ + t * 16 + (lane & 15)) * (2 * DM) + br * DM + cb * 64 + m * 32 + (lane >> 4) * 4) * 2;
  *(u32x2*)dst = u32x2{pk2(s[0], s[1]), pk2(s[2], s[3])};
  *(u32x2*)(dst + 32) = u32x2{pk2(s[4], s[5]), pk2(s[6], s[7])};
}

__global__ __launch_bounds__(512, 2)
void gqa_main(const char* __restrict__ xA, const char* __restrict__ kA, const char* __restrict__ vA, const char* __restrict__ wqA, const char* __restrict__ woA,
              const char* __restrict__ xB, const char* __restrict__ kB, const char* __restrict__ vB, const char* __restrict__ wqB, const char* __restrict__ woB,
              char* ws, char* out) {
  __shared__ __attribute__((aligned(16))) char smem_raw[LDS_TOTAL];
  const lptr smem = (lptr)smem_raw;
  const unsigned ldsb = (unsigned)(uintptr_t)smem;
  const int tid = threadIdx.x;
  const int wid = __builtin_amdgcn_readfirstlane(tid >> 6);
  const int bid = blockIdx.x, xcd = bid & 7, jj = bid >> 3;
  const int br = xcd >> 2, b = jj >> 2, kvh = 2 * (xcd & 3) + ((jj >> 1) & 1), h = 2 * kvh + (jj & 1);
  const int grp = br * NB + b;
  const char* const xb = (br ? xB : xA) + (size_t)b * SQ * DM * 2;
  const char* const wqh = (br ? wqB : wqA) + h * DH * 2;
  const char* const woh = (br ? woB : woA) + (size_t)h * DH * DM * 2;
  const rsrc_t srdWo = mk_srd(woh + wid * 128, DH * DM * 2 - wid * 128);
  const rsrc_t srdP = mk_srd(ws + WS_PART + ((size_t)(grp * HQ + h) * NBLK + wid * 10) * 1024, 10 * 1024);
  constexpr unsigned WOSLOT = 64 * DM * 2, WOP = 8 * DM * 2, WQROW = HQ * DH * 2;
  unsigned long long tag = 0;
  STAMP_DECL;
  STAMP(0);

  if (wid < 4) {
    const int w = wid;
    const int lane = tid & 63;
    {
      const int id = 8 * (4 * lane + w) + b;
      touch(ldsb + LDS_SCR, lane < 56 ? (unsigned)((id >> 2) * WQROW + (id & 3) * 128) : OOB, mk_srd(wqh, DM * WQROW - h * DH * 2));
    }
    __builtin_amdgcn_s_setprio(1);
    const rsrc_t srdK = mk_srd((br ? kB : kA) + ((size_t)(b * HKV + kvh) * SK + w * 16) * (DH * 2), (SK - w * 16) * DH * 2);
    const unsigned kring = ldsb + w * KRING;
    const unsigned kv = (lane >> 5) * 512 + (((lane & 31) ^ (lane >> 5)) * 16);
    {
      u32x4 xr[7][3];
      const rsrc_t srdX = mk_srd(xb, SQ * DM * 2);
      const unsigned xo = (8 * w + (lane >> 3)) * (DM * 2) + (lane & 7) * 16;
#pragma unroll
      for (int j = 0; j < 7; ++j)
#pragma unroll
        for (int i = 0; i < 3; ++i)
          asm volatile("buffer_load_dwordx4 %0, %1, %2, %3 offen offset:%4" : "=v"(xr[j][i]) : "v"(xo), "s"(srdX), "s"(i * 32 * DM * 2), "i"(j * 128) : "memory");
      if (lane < 8) *(LAS unsigned*)(smem + LDS_CHG + lane * 4) = 0;
      const int xg = lane & 3, xk = (lane >> 2) & 1;
      const lptr xdst = smem + LDS_P + (xk * 5 + (w >> 1)) * 1024 + 16 * ((8 * (w & 1) + (lane >> 3) + 16 * xg) ^ (2 * xg + xk));
#define XWAIT(j, n) asm volatile("s_waitcnt vmcnt(" #n ")" : "+v"(xr[j][0]), "+v"(xr[j][1]), "+v"(xr[j][2]) :: "memory")
#define XWRITE(j) do { *(LAS u32x4*)(xdst + ((j) & 1) * PBUF) = xr[j][0]; *(LAS u32x4*)(xdst + ((j) & 1) * PBUF + 2048) = xr[j][1]; \
                       if (w < 2) *(LAS u32x4*)(xdst + ((j) & 1) * PBUF + 4096) = xr[j][2]; } while (0)
#define KQUAD(n) do { _Pragma("unroll") for (int p = 0; p < 4; ++p) dma16<KV_NT>(kring + ((n) >> 1) * KSLOT + (((n) & 1) * 4 + p) * KPIECE, kv, srdK, ((n) >> 1) * (64 * DH * 2) + (((n) & 1) * 4 + p) * 1024); } while (0)
      XWAIT(0, 18); XWRITE(0);
      XWAIT(1, 15); XWRITE(1);
      STAMP(1);
      WAIT_LGKM0(); BAR();
      STAMP(2);
      BAR(); XWAIT(2, 12); XWRITE(2); KQUAD(0); WAIT_LGKM0();
      BAR(); XWAIT(3, 13); XWRITE(3); KQUAD(1); WAIT_LGKM0();
      BAR(); XWAIT(4, 14); XWRITE(4); KQUAD(2); WAIT_LGKM0();
      BAR(); XWAIT(5, 15); XWRITE(5); KQUAD(3); WAIT_LGKM0();
      BAR(); XWAIT(6, 16); XWRITE(6); WAIT_LGKM0();
      BAR();
      BAR();
#undef XWAIT
#undef XWRITE
#undef KQUAD
    }
    BAR();
    STAMP(3);
    bf16x8 qf[8][5];
#pragma unroll
    for (int ks = 0; ks < 8; ++ks)
#pragma unroll
      for (int t = 0; t < 5; ++t) qf[ks][t] = ld128(smem + LDS_X + (3 - (ks >> 1)) * PBUF + ((ks & 1) * 5 + t) * 1024 + lane * 16);
    WAIT_LGKM0(); BAR();
    STAMP(4);
    float ref[5], l[5];
#pragma unroll
    for (int t = 0; t < 5; ++t) { ref[t] = NEG; l[t] = 0.f; }
    const lptr ka0 = smem + w * KRING + ((lane & 15) >> 1) * KPIECE + (lane & 1) * 512 + (((lane >> 4) ^ (lane & 1)) * 16);
    const lptr pw0 = smem + LDS_P + (w >> 1) * 5120 + (w & 1) * 512 + 256 * (lane >> 5) + 16 * (lane & 15) + 8 * ((lane >> 4) & 1);
    auto adopt = [&](int par) {
      const u32x4 ch = *(LAS u32x4*)(smem + LDS_CHG + par * 16);
      if (__builtin_amdgcn_readfirstlane(ch[0] | ch[1] | ch[2] | ch[3])) {
        const lptr rb = smem + LDS_REF + par * 1280 + (opaque(lane) & 15) * 4;
#pragma unroll
        for (int t = 0; t < 5; ++t) {
          float m = ref[t];
#pragma unroll
          for (int ww = 0; ww < 4; ++ww) { float r = *(LAS float*)(rb + ww * 320 + t * 64); asm volatile("" : "+v"(r)); m = fmaxf(m, ch[ww] ? r : NEG); }
          const float f = ex2(ref[t] - m);
          l[t] *= f; ref[t] = m;
          LAS u32x2* pp = (LAS u32x2*)(pw0 + par * PBUF + t * 1024);
          const u32x2 pv = *pp;
          *pp = u32x2{pk2(blo(pv[0]) * f, bhi(pv[0]) * f), pk2(blo(pv[1]) * f, bhi(pv[1]) * f)};
          SCHED_FENCE();
        }
        WAIT_LGKM0(); BAR();
      }
    };
    auto tile = [&](int T, auto mode) {
      constexpr int MODE = decltype(mode)::value;
      SEG0();
      WAIT_VM(8);
      SEG(0);
      const lptr ka = ka0 + (T & 1) * KSLOT;
      bf16x8 kf[8];
#pragma unroll
      for (int i = 0; i < 8; ++i) kf[i] = ld128(ka + 64 * i);
      adopt((T + 1) & 1);
      WAIT_LGKM0();
      SEG(1);
      unsigned we = 0;
      if constexpr (MODE != 0) we = row_src(opaque(lane), DM * 2);
      f32x4 s[5];
#pragma unroll
      for (int t = 0; t < 5; ++t) s[t] = f32x4{0.f, 0.f, 0.f, 0.f};
#pragma unroll
      for (int i = 0; i < 8; ++i) {
#pragma unroll
        for (int t = 0; t < 5; ++t) s[t] = mfma(kf[i], qf[i][t], s[t]);
        SCHED_FENCE();
        if constexpr (MODE == 0) dma16<KV_NT>(kring + (T & 1) * KSLOT + i * KPIECE, kv, srdK, (T + 2) * (64 * DH * 2) + i * 1024);
        else dma_row_piece<W_NT>(i, kring + (MODE - 1) * KSLOT, we, we ^ 64, srdWo, (MODE - 1) * WOSLOT, WOP);
        SCHED_FENCE();
      }
      float mx = NEG;
#pragma unroll
      for (int t = 0; t < 5; ++t) mx = fmaxf(mx, fmaf(fmaxf(fmaxf(s[t][0], s[t][1]), fmaxf(s[t][2], s[t][3])), SCL, -ref[t]));
      unsigned changed = 0;
      if (__builtin_amdgcn_ballot_w64(mx > THR)) {
        changed = 1;
        const int ln = opaque(lane);
        const lptr rb = smem + LDS_REF + ((T & 1) * 4 + w) * 320 + (ln & 15) * 4;
#pragma unroll
        for (int t = 0; t < 5; ++t) {
          const float m = red_max_g(fmaxf(fmaxf(s[t][0], s[t][1]), fmaxf(s[t][2], s[t][3])));
          const float nr = fmaxf(ref[t], ceilf(m * SCL));
          l[t] *= ex2(ref[t] - nr); ref[t] = nr;
          if (ln < 16) *(LAS float*)(rb + t * 64) = nr;
          SCHED_FENCE();
        }
      }
      const lptr pw = pw0 + (T & 1) * PBUF;
#pragma unroll
      for (int t = 0; t < 5; ++t) {
        f32x4 p;
#pragma unroll
        for (int r = 0; r < 4; ++r) p[r] = ex2(fmaf(s[t][r], SCL, -ref[t]));
        l[t] += (p[0] + p[1]) + (p[2] + p[3]);
        *(LAS u32x2*)(pw + t * 1024) = pk4(p);
      }
      if (lane == 0) *(LAS unsigned*)(smem + LDS_CHG + ((T & 1) * 4 + w) * 4) = changed;
      WAIT_LGKM0();
      SEG(2);
      BAR();
      SEG(3);
    };
    for (int T = 0; T < NTILE - 2; ++T) { tile(T, IC<0>{}); if (T == 1) STAMP(5); if (T == 7) STAMP(6); }
    tile(NTILE - 2, IC<1>{});
    tile(NTILE - 1, IC<2>{});
    STAMP(7);
    adopt(1);
    const int ln = opaque(lane);
#pragma unroll
    for (int t = 0; t < 5; ++t) { const float ls = red_sum_g(l[t]); if (ln < 16) *(LAS float*)(smem + LDS_L + (w * 80 + t * 16 + ln) * 4) = ls; }
    SEG0();
    WAIT_LGKM0(); BAR();
    SEG(8);
    bf16x8 wf[8][4];
    const lptr wring = smem + w * KRING;
    const RowImg ri = row_img(ln);
    const unsigned we = row_src(ln, DM * 2);
    WAIT_VM(8);
    ld_slot(wf + 0, wring, ri);
    WAIT_LGKM0();
    dma_rows<W_NT>(kring, we, we ^ 64, srdWo, 2 * WOSLOT, WOP);
    WAIT_VM(8);
    ld_slot(wf + 2, wring + KSLOT, ri);
    WAIT_LGKM0();
    dma_rows<W_NT>(kring + KSLOT, we, we ^ 64, srdWo, 3 * WOSLOT, WOP);
    WAIT_VM(8);
    ld_slot(wf + 4, wring, ri);
    WAIT_VM(0);
    ld_slot(wf + 6, wring + KSLOT, ri);
    SEG(9);
    if (FUSED) tag = __hip_atomic_load((gu64*)(ws + grp * 4096), __ATOMIC_RELAXED, __HIP_MEMORY_SCOPE_AGENT) + 1;
    BAR();
    SEG(10);
    STAMP(8);
    out_proj(wf, smem, srdP, ln);
    STAMP(10);
  } else {
    const int v = wid - 4;
    const int lane = tid & 63;
    {
      const int k = 3 * lane + v, id = 4 * k + (jj & 3);
      touch(ldsb + LDS_SCR, (v < 3 && k < 140) ? (unsigned)((id % 80) * (DM * 2) + (id / 80) * 128) : OOB, mk_srd(xb, SQ * DM * 2));
    }
    const rsrc_t srdQ = mk_srd(wqh + v * 128, DM * WQROW - h * DH * 2 - v * 128);
    const rsrc_t srdV = mk_srd((br ? vB : vA) + (size_t)(b * HKV + kvh) * SK * DH * 2 + v * 128, SK * DH * 2 - v * 128);
    const unsigned vring = ldsb + LDS_V + v * VRING;
    const unsigned wqe = row_src(lane, WQROW), ve = row_src(lane, DH * 2), vo = ve ^ 64;
    dma_rows<W_NT>(vring, wqe, wqe ^ 64, srdQ, 0, 8 * WQROW);
    dma_rows<W_NT>(vring + VSLOT, wqe, wqe ^ 64, srdQ, 64 * WQROW, 8 * WQROW);
    const RowImg ri = row_img(lane);
    const lptr wring = smem + LDS_V + v * VRING;
    f32x4 acc[4][5];
#pragma unroll
    for (int u = 0; u < 4; ++u)
#pragma unroll
      for (int t = 0; t < 5; ++t) acc[u][t] = f32x4{0.f, 0.f, 0.f, 0.f};
    BAR();
    STAMP(2);
    {
      const lptr xr0 = smem + LDS_P + 16 * (lane ^ (2 * (lane >> 4))), xr1 = smem + LDS_P + 5120 + 16 * (lane ^ (2 * (lane >> 4) + 1));
      bf16x8 pf[2][2][5];
#pragma unroll
      for (int t = 0; t < 5; ++t) { pf[0][0][t] = ld128(xr0 + t * 1024); pf[0][1][t] = ld128(xr1 + t * 1024); }
#pragma unroll
      for (int j = 0; j < 7; ++j) {
        SEG0();
        WAIT_LGKM0(); BAR();
        SEG(4);
        if (j == 6 && v == 3) WAIT_VM(0); else WAIT_VM(8);
        SEG(5);
        bf16x8 af[2][4];
        ld_slot(af, wring + (j & 1) * VSLOT, ri);
        WAIT_LGKM0();
        SEG(6);
        if (j < 6) {
#pragma unroll
          for (int t = 0; t < 5; ++t) { pf[(j + 1) & 1][0][t] = ld128(xr0 + ((j + 1) & 1) * PBUF + t * 1024); pf[(j + 1) & 1][1][t] = ld128(xr1 + ((j + 1) & 1) * PBUF + t * 1024); }
        }
#pragma unroll
        for (int i = 0; i < 8; ++i) {
#pragma unroll
          for (int t = 0; t < 5; ++t) acc[i & 3][t] = mfma(af[i >> 2][i & 3], pf[j & 1][i >> 2][t], acc[i & 3][t]);
          SCHED_FENCE();
          if (j + 2 <= 6) dma_row_piece<W_NT>(i, vring + (j & 1) * VSLOT, wqe, wqe ^ 64, srdQ, (j + 2) * (64 * WQROW), 8 * WQROW);
          else if (v < 3) dma_row_piece<KV_NT>(i, vring + (j & 1) * VSLOT, ve, vo, srdV, (j - 5) * (64 * DH * 2), 8 * DH * 2);
          SCHED_FENCE();
        }
        SEG(7);
      }
    }
    const lptr xo = smem + LDS_X + (3 - v) * PBUF + 256 * (lane >> 5) + 16 * (lane & 15) + 8 * ((lane >> 4) & 1);
#pragma unroll
    for (int u = 0; u < 4; ++u)
#pragma unroll
      for (int t = 0; t < 5; ++t) { *(LAS u32x2*)(xo + (u >> 1) * 5120 + (u & 1) * 512 + t * 1024) = pk4(acc[u][t]); acc[u][t] = f32x4{0.f, 0.f, 0.f, 0.f}; }
    STAMP(3);
    WAIT_LGKM0(); BAR();
    BAR();
    STAMP(4);
    if (v == 3) {
      dma_rows<KV_NT>(vring + VSLOT, ve, vo, srdV, 0, 8 * DH * 2);
      dma_rows<KV_NT>(vring, ve, vo, srdV, 64 * DH * 2, 8 * DH * 2);
    }
    const lptr pr0 = smem + LDS_P + lane * 16;
    float mo[5];
#pragma unroll
    for (int t = 0; t < 5; ++t) mo[t] = NEG;
    BAR();
    auto tile = [&](int T, auto mode) {
      constexpr int MODE = decltype(mode)::value;
      SEG0();
      if (MODE == 2 && v == 3) WAIT_VM(0); else WAIT_VM(8);
      SEG(0);
      const int vs = ((T + 1) & 1) * VSLOT;
      const lptr pr = pr0 + (T & 1) * PBUF;
      bf16x8 af[2][4], pf[2][5];
      const u32x4 ch = *(LAS u32x4*)(smem + LDS_CHG + (T & 1) * 16);
      ld_slot(af, wring + vs, ri);
      if (__builtin_amdgcn_readfirstlane(ch[0] | ch[1] | ch[2] | ch[3])) {
        const lptr rb = smem + LDS_REF + (T & 1) * 1280 + (opaque(lane) & 15) * 4;
#pragma unroll
        for (int t = 0; t < 5; ++t) {
          float mn = mo[t];
#pragma unroll
          for (int ww = 0; ww < 4; ++ww) { float r = *(LAS float*)(rb + ww * 320 + t * 64); asm volatile("" : "+v"(r)); mn = fmaxf(mn, ch[ww] ? r : NEG); }
          const float fo = ex2(mo[t] - mn);
          mo[t] = mn;
#pragma unroll
          for (int u = 0; u < 4; ++u) acc[u][t] *= fo;
          SCHED_FENCE();
        }
        BAR();
      }
#pragma unroll
      for (int ks = 0; ks < 2; ++ks)
#pragma unroll
        for (int t = 0; t < 5; ++t) pf[ks][t] = ld128(pr + (ks * 5 + t) * 1024);
      WAIT_LGKM0();
      SEG(1);
      unsigned we = 0;
      if constexpr (MODE != 0) we = row_src(opaque(lane), DM * 2);
      if constexpr (MODE == 2) BAR();
      if constexpr (MODE == 0) {
        if (T == 4) {
          const int ln = opaque(lane);
          touch(ldsb + LDS_SCR, ln < 56 ? (unsigned)((8 * (4 * ln + v) + b) * 128) : OOB, mk_srd(woh, DH * DM * 2));
        }
      }
#pragma unroll
      for (int i = 0; i < 8; ++i) {
#pragma unroll
        for (int t = 0; t < 5; ++t) acc[i & 3][t] = mfma(af[i >> 2][i & 3], pf[i >> 2][t], acc[i & 3][t]);
        SCHED_FENCE();
        if constexpr (MODE == 0) dma_row_piece<KV_NT>(i, vring + vs, ve, vo, srdV, (T + 2) * (64 * DH * 2), 8 * DH * 2);
        else if (v < 3) dma_row_piece<W_NT>(i, vring + vs, we, we ^ 64, srdWo, (MODE - 1) * WOSLOT, WOP);
        SCHED_FENCE();
      }
      SEG(2);
      if constexpr (MODE != 2) BAR();
      SEG(3);
    };
    for (int T = 0; T < NTILE - 2; ++T) { tile(T, IC<0>{}); if (T == 0) STAMP(5); if (T == 6) STAMP(6); }
    tile(NTILE - 2, IC<1>{});
    STAMP(7);
    tile(NTILE - 1, IC<2>{});
    SEG0();
    const int ln = opaque(lane);
#pragma unroll
    for (int t = 0; t < 5; ++t) {
      float ls = 0.f;
#pragma unroll
      for (int ww = 0; ww < 4; ++ww) ls += *(LAS float*)(smem + LDS_L + (ww * 80 + t * 16 + (ln & 15)) * 4);
      const float inv = 1.0f / ls;
#pragma unroll
      for (int u = 0; u < 4; ++u) *(LAS u32x2*)(xo + (u >> 1) * 5120 + (u & 1) * 512 + t * 1024) = pk4(acc[u][t] * inv);
    }
    WAIT_LGKM0();
    SEG(9);
    BAR();
    SEG(10);
    STAMP(8);
    if (v < 3) {
      bf16x8 wf[8][4];
      const unsigned we = row_src(ln, DM * 2);
      WAIT_VM(8);
      ld_slot(wf + 0, wring + VSLOT, ri);
      WAIT_LGKM0();
      dma_rows<W_NT>(vring + VSLOT, we, we ^ 64, srdWo, 2 * WOSLOT, WOP);
      WAIT_VM(8);
      ld_slot(wf + 2, wring, ri);
      WAIT_LGKM0();
      dma_rows<W_NT>(vring, we, we ^ 64, srdWo, 3 * WOSLOT, WOP);
      WAIT_VM(8);
      ld_slot(wf + 4, wring + VSLOT, ri);
      WAIT_VM(0);
      ld_slot(wf + 6, wring, ri);
      if (FUSED) tag = __hip_atomic_load((gu64*)(ws + grp * 4096), __ATOMIC_RELAXED, __HIP_MEMORY_SCOPE_AGENT) + 1;
      STAMP(9);
      out_proj(wf, smem, srdP, ln);
      STAMP(10);
    }
  }
  WAIT_VM(0);
  STAMP(11);
  BAR();
  {
    const int lane = opaque(tid) & 63;
    gu64* const ctl = (gu64*)(ws + grp * 4096);
    if (wid == 0 && lane == 0) __hip_atomic_store(ctl + 16 * (1 + h), tag, __ATOMIC_RELAXED, __HIP_MEMORY_SCOPE_AGENT);
    const int blk = h + 16 * wid;
    if (wid < 5 && blk < NBLK) {
      for (unsigned spins = 0; spins < (1u << 18); ++spins) {
        const unsigned long long f = __hip_atomic_load(ctl + 16 * (1 + (lane & 15)), __ATOMIC_RELAXED, __HIP_MEMORY_SCOPE_AGENT);
        if (__builtin_amdgcn_ballot_w64(f != tag) == 0) break;
        __builtin_amdgcn_s_sleep(1);
      }
      __builtin_amdgcn_fence(__ATOMIC_ACQUIRE, "wavefront");
      STAMP(12);
      if (h == 0 && wid == 0 && lane == 0) __hip_atomic_store(ctl, tag, __ATOMIC_RELAXED, __HIP_MEMORY_SCOPE_AGENT);
      reduce_block<16>(ws, out, grp, blk, lane);
      STAMP(13);
    }
  }
  STAMP_FLUSH();
}

extern "C" void kernel_launch(void* const* d_in, const int* in_sizes, int n_in, void* d_out, int out_size, void* d_ws, size_t ws_size,
                              hipStream_t stream) {
  (void)in_sizes; (void)n_in; (void)out_size; (void)ws_size;
  gqa_main<<<256, 512, 0, stream>>>((const char*)d_in[0], (const char*)d_in[1], (const char*)d_in[2], (const char*)d_in[3], (const char*)d_in[4],
                                    (const char*)d_in[5], (const char*)d_in[6], (const char*)d_in[7], (const char*)d_in[8], (const char*)d_in[9],
                                    (char*)d_ws, (char*)d_out);
}
```

```cpp
#include <hip/hip_runtime.h>
#include <hip/hip_bf16.h>
#include <stdint.h>

typedef __bf16 bf16x8 __attribute__((ext_vector_type(8)));
typedef __bf16 bf16x2 __attribute__((ext_vector_type(2)));
typedef short s16x4 __attribute__((ext_vector_type(4)));
typedef float f32x4 __attribute__((ext_vector_type(4)));
typedef float f32x2 __attribute__((ext_vector_type(2)));
typedef unsigned u32x4 __attribute__((ext_vector_type(4)));
typedef unsigned u32x2 __attribute__((ext_vector_type(2)));
#define LAS __attribute__((address_space(3)))
#define GAS __attribute__((address_space(1)))
typedef LAS char* lptr;
typedef GAS unsigned long long gu64;
typedef __amdgpu_buffer_rsrc_t rsrc_t;

constexpr int NB = 8, SQ = 80, DM = 448, HQ = 16, HKV = 8, SK = 1024, DH = 256;
constexpr int NTILE = SK / 64;
constexpr int PIECE = 1056, HALF = 4 * PIECE, SLOT = 8 * PIECE, RING = 2 * SLOT;
constexpr int LDS_X = 7 * RING;
constexpr int LDS_P = LDS_X + 20480, PBUF = 10240;
constexpr int LDS_REF = LDS_X + 40960;
constexpr int LDS_CHG = LDS_REF + 2560;
constexpr int LDS_L = LDS_CHG + 32;
constexpr int LDS_EP = LDS_L + 1280;
constexpr int LDS_SCR = LDS_EP + 64;
constexpr int LDS_TOTAL = 163840;
static_assert(LDS_SCR + 256 <= LDS_TOTAL && LDS_EP % 16 == 0 && RING <= 20480, "lds");

constexpr float SCL = 0.0625f * 1.4426950408889634f;
constexpr float THR = 10.f;
constexpr float NEG = -1.0e30f;
constexpr int NBLK = 70;
constexpr size_t WS_PART = 1 << 20;
constexpr unsigned OOB = 0x7ffffff0u;

#define WAIT_VM(n) asm volatile("s_waitcnt vmcnt(" #n ")" ::: "memory")
#define WAIT_LGKM0() do { asm volatile("" ::: "memory"); __builtin_amdgcn_s_waitcnt(0xC07F); asm volatile("" ::: "memory"); } while (0)
#define UNLIKELY(x) __builtin_expect(!!(x), 0)
#define LIKELY(x) __builtin_expect(!!(x), 1)
#define SCHED_FENCE() __builtin_amdgcn_sched_barrier(0)
#define MIX5() do { _Pragma("unroll") for (int i_ = 0; i_ < 5; ++i_) { __builtin_amdgcn_sched_group_barrier(0x8, 1, 0); __builtin_amdgcn_sched_group_barrier(0x100, 1, 0); } } while (0)
#define BAR() do { SCHED_FENCE(); asm volatile("" ::: "memory"); __builtin_amdgcn_s_barrier(); asm volatile("" ::: "memory"); SCHED_FENCE(); } while (0)

#define STAMP(i) do {} while (0)
#define STAMP_DECL do {} while (0)
#define STAMP_FLUSH() do {} while (0)

#define DMA_POL "nt"
template <int P>
__device__ __forceinline__ void dma_piece(unsigned m0v, unsigned voff, rsrc_t srd, unsigned soff) {
  if (P == 0) asm volatile("s_mov_b32 m0, %0\n\ts_nop 0\n\tbuffer_load_dwordx4 %1, %2, %3 offen " DMA_POL " lds" :: "s"(m0v), "v"(voff), "s"(srd), "s"(soff) : "memory");
  else asm volatile("buffer_load_dwordx4 %0, %1, %2 offen offset:%3 " DMA_POL " lds" :: "v"(voff), "s"(srd), "s"(soff), "i"(P * PIECE) : "memory");
}
__device__ __forceinline__ void dma_half(unsigned m0v, unsigned voff, rsrc_t srd, unsigned soff, unsigned ps) {
  dma_piece<0>(m0v, voff, srd, soff); dma_piece<1>(m0v, voff, srd, soff + ps); dma_piece<2>(m0v, voff, srd, soff + 2 * ps); dma_piece<3>(m0v, voff, srd, soff + 3 * ps);
}
__device__ __forceinline__ void touch(unsigned m0v, unsigned voff, rsrc_t srd) {
  asm volatile("s_mov_b32 m0, %0\n\ts_nop 0\n\tbuffer_load_dword %1, %2, 0 offen lds" :: "s"(m0v), "v"(voff), "s"(srd) : "memory");
}
__device__ __forceinline__ bf16x8 ld128(lptr p) { return *(LAS bf16x8*)p; }
__device__ __forceinline__ bf16x8 ldtr(lptr p) {
  s16x4 a = __builtin_amdgcn_ds_read_tr16_b64_v4i16((LAS s16x4*)p);
  s16x4 b = __builtin_amdgcn_ds_read_tr16_b64_v4i16((LAS s16x4*)(p + 512));
  return __builtin_bit_cast(bf16x8, __builtin_shufflevector(a, b, 0, 1, 2, 3, 4, 5, 6, 7));
}
__device__ __forceinline__ unsigned pk2(float a, float b) { f32x2 t = {a, b}; return __builtin_bit_cast(unsigned, __builtin_convertvector(t, bf16x2)); }
__device__ __forceinline__ u32x2 pk4(f32x4 v) { u32x2 r = {pk2(v[0], v[1]), pk2(v[2], v[3])}; return r; }
__device__ __forceinline__ float blo(unsigned u) { return __uint_as_float(u << 16); }
__device__ __forceinline__ float bhi(unsigned u) { return __uint_as_float(u & 0xffff0000u); }
__device__ __forceinline__ f32x4 mfma(bf16x8 a, bf16x8 b, f32x4 c) { return __builtin_amdgcn_mfma_f32_16x16x32_bf16(a, b, c, 0, 0, 0); }
__device__ __forceinline__ float ex2(float x) { return __builtin_amdgcn_exp2f(x); }
__device__ __forceinline__ float max2(float a, float b) { return __builtin_elementwise_maximum(a, b); }
__device__ __forceinline__ float max3(float a, float b, float c) { return max2(max2(a, b), c); }
__device__ __forceinline__ float max4(f32x4 v) { return max2(max3(v[0], v[1], v[2]), v[3]); }
__device__ __forceinline__ float red_max_g(float v) {
  auto a = __builtin_amdgcn_permlane16_swap(__float_as_uint(v), __float_as_uint(v), false, false);
  v = max2(__uint_as_float(a[0]), __uint_as_float(a[1]));
  auto b = __builtin_amdgcn_permlane32_swap(__float_as_uint(v), __float_as_uint(v), false, false);
  return max2(__uint_as_float(b[0]), __uint_as_float(b[1]));
}
__device__ __forceinline__ float red_sum_g(float v) {
  auto a = __builtin_amdgcn_permlane16_swap(__float_as_uint(v), __float_as_uint(v), false, false);
  v = __uint_as_float(a[0]) + __uint_as_float(a[1]);
  auto b = __builtin_amdgcn_permlane32_swap(__float_as_uint(v), __float_as_uint(v), false, false);
  return __uint_as_float(b[0]) + __uint_as_float(b[1]);
}
template <int V> struct IC { static constexpr int value = V; };
__device__ __forceinline__ int opaque(int v) { asm volatile("" : "+v"(v)); return v; }
__device__ __forceinline__ rsrc_t mk_srd(const void* p, unsigned bytes) { return __builtin_amdgcn_make_buffer_rsrc((void*)p, (short)0, (int)bytes, 0x00020000); }
__device__ __forceinline__ unsigned row_src(int lane, int stride) { const int srow = lane >> 3; return srow * stride + (((lane & 7) ^ (((srow >> 1) & 1) << 2)) * 16); }
__device__ __forceinline__ void ld_refs(float (*rr)[4], lptr rb) {
#pragma unroll
  for (int t = 0; t < 5; ++t)
#pragma unroll
    for (int ww = 0; ww < 4; ++ww) rr[t][ww] = *(LAS float*)(rb + ww * 320 + t * 64);
#pragma unroll
  for (int t = 0; t < 5; ++t) asm volatile("" : "+v"(rr[t][0]), "+v"(rr[t][1]), "+v"(rr[t][2]), "+v"(rr[t][3]));
}
__device__ __forceinline__ int frag_wr(int lane) { return 16 * ((lane & 15) ^ (2 * (lane >> 5))) + 256 * (lane >> 5) + 8 * ((lane >> 4) & 1); }
__device__ __forceinline__ int frag_wr_u(int a0, int ulo, int uhi) { return (a0 ^ (16 * (4 * ulo + uhi))) + 512 * ulo + 5120 * uhi; }

__global__ __launch_bounds__(512, 2)
void gqa_main(const char* __restrict__ xA, const char* __restrict__ kA, const char* __restrict__ vA, const char* __restrict__ wqA, const char* __restrict__ woA,
              const char* __restrict__ xB, const char* __restrict__ kB, const char* __restrict__ vB, const char* __restrict__ wqB, const char* __restrict__ woB,
              char* ws, char* out) {
  __shared__ __attribute__((aligned(16))) char smem_raw[LDS_TOTAL];
  asm volatile(".Lgqa_beg:");
  const lptr smem = (lptr)smem_raw;
  const unsigned ldsb = (unsigned)(uintptr_t)smem;
  const int tid = threadIdx.x;
  const int wid = __builtin_amdgcn_readfirstlane(tid >> 6);
  const int bid = blockIdx.x, xcd = bid & 7, jj = bid >> 3;
  const int br = xcd >> 2, b = jj >> 2, kvh = 2 * (xcd & 3) + ((jj >> 1) & 1), h = 2 * kvh + (jj & 1);
  const int grp = br * NB + b;
  const char* const xb = (br ? xB : xA) + (size_t)b * SQ * DM * 2;
  const char* const wqh = (br ? wqB : wqA) + h * DH * 2;
  const char* const woh = (br ? woB : woA) + (size_t)h * DH * DM * 2;
  const char* const vbase = (br ? vB : vA) + (size_t)(b * HKV + kvh) * SK * DH * 2;
  const rsrc_t srdWo = mk_srd(woh + wid * 128, wid < 7 ? DH * DM * 2 - wid * 128 : 0);
  constexpr unsigned WOSLOT = 64 * DM * 2, WOP = 8 * DM * 2, WQROW = HQ * DH * 2, KVTILE = 64 * DH * 2;
  const unsigned ring = wid * RING;
  STAMP_DECL;
  STAMP(0);

  const int v = wid - 4;

  if (wid < 4) {
    const int w = wid;
    const int lane = tid & 63;
    __builtin_amdgcn_s_setprio(1);
    const rsrc_t srdK = mk_srd((br ? kB : kA) + ((size_t)(b * HKV + kvh) * SK + w * 16) * (DH * 2) - 128, (SK - w * 16) * DH * 2 + 128);
    const unsigned kv = (lane >> 5) * 512 + (((lane & 31) ^ (lane >> 5)) * 16);
    constexpr unsigned KPS = 1024 - PIECE;
#define ABL_K srdK
    {
      u32x4 xr[7][3];
      const rsrc_t srdX = mk_srd(xb, SQ * DM * 2);
      const unsigned xo = (8 * w + (lane >> 3)) * (DM * 2) + (lane & 7) * 16;
#define XLOAD(j) do { _Pragma("unroll") for (int i = 0; i < 3; ++i) \
        asm volatile("buffer_load_dwordx4 %0, %1, %2, %3 offen offset:%4" : "=v"(xr[j][i]) : "v"(xo), "s"(srdX), "s"(i * 32 * DM * 2), "i"((j) * 128) : "memory"); } while (0)
#define XWAIT(j, n) asm volatile("s_waitcnt vmcnt(" #n ")" : "+v"(xr[j][0]), "+v"(xr[j][1]), "+v"(xr[j][2]) :: "memory")
#define XWRITE(j) do { *(LAS u32x4*)(xdst + ((j) & 1) * PBUF) = xr[j][0]; *(LAS u32x4*)(xdst + ((j) & 1) * PBUF + 2048) = xr[j][1]; \
                       if (w < 2) *(LAS u32x4*)(xdst + ((j) & 1) * PBUF + 4096) = xr[j][2]; } while (0)
#define KHALF(n) dma_half(ldsb + ring + (n) * HALF, kv, ABL_K, 128 + ((n) >> 1) * KVTILE + ((n) & 1) * 4096, KPS)
      XLOAD(0); XLOAD(1);
      if (lane < 8) *(LAS unsigned*)(smem + LDS_CHG + lane * 4) = 0;
      const int xg = lane & 3, xk = (lane >> 2) & 1;
      const lptr xdst = smem + LDS_P + (xk * 5 + (w >> 1)) * 1024 + 16 * ((8 * (w & 1) + (lane >> 3) + 16 * xg) ^ (2 * xg + xk));
      XWAIT(0, 3); XWRITE(0);
      XWAIT(1, 0); XWRITE(1);
      STAMP(1);
      WAIT_LGKM0(); BAR();
      STAMP(2);
      {
        const int id = 512 + 8 * (4 * lane + w) + b;
        touch(ldsb + LDS_SCR, lane < 40 ? (unsigned)((id >> 2) * WQROW + (id & 3) * 128) : OOB, mk_srd(wqh, DM * WQROW - h * DH * 2));
      }
      if (w < 3) {
        const int k = 3 * lane + w, id = 160 + 4 * k + (jj & 3);
        touch(ldsb + LDS_SCR, k < 100 ? (unsigned)((id % 80) * (DM * 2) + (id / 80) * 128) : OOB, srdX);
      } else {
        unsigned long long pc; unsigned cb_, ce_;
        asm volatile("s_getpc_b64 %0\n.Lgqa_pc:\n\ts_mov_b32 %1, .Lgqa_pc-.Lgqa_beg\n\ts_mov_b32 %2, .Lgqa_end-.Lgqa_beg" : "=s"(pc), "=s"(cb_), "=s"(ce_));
        const unsigned co = (jj * 8 + (lane - 8)) * 128;
        const char* a = lane < 2 ? ws + grp * 4096 + lane * 4 : lane == 2 ? vbase : lane == 3 ? woh : lane == 4 ? ws + WS_PART + (size_t)grp * NBLK * 16384
                      : lane == 5 ? ws + WS_PART + (size_t)(grp + 1) * NBLK * 16384 - 128 : lane == 6 ? out + (size_t)b * SQ * 4 * DM : lane == 7 ? (br ? kB : kA) + (size_t)(b * HKV + kvh) * SK * DH * 2
                      : (const char*)(pc - cb_) + (co < ce_ ? co : 0u);
        if (lane < 16) asm volatile("s_mov_b32 m0, %0\n\ts_nop 0\n\tglobal_load_lds_dword %1, off" :: "s"(ldsb + LDS_EP), "v"(a) : "memory");
      }
      XLOAD(2); XLOAD(3); XLOAD(4); XLOAD(5); XLOAD(6);
      BAR(); XWAIT(2, 12); XWRITE(2); KHALF(0); WAIT_LGKM0();
      BAR(); XWAIT(3, 13); XWRITE(3); KHALF(1); WAIT_LGKM0();
      BAR(); XWAIT(4, 14); XWRITE(4); KHALF(2); WAIT_LGKM0();
      BAR(); XWAIT(5, 15); XWRITE(5); KHALF(3); WAIT_LGKM0();
      BAR(); XWAIT(6, 16); XWRITE(6); WAIT_LGKM0();
      touch(ldsb + LDS_SCR, lane < 56 ? (unsigned)((8 * (4 * lane + w) + b) * 128) : OOB, mk_srd(woh, DH * DM * 2));
      BAR();
      BAR();
#undef XLOAD
#undef XWAIT
#undef XWRITE
#undef KHALF
    }
    const lptr ka0 = smem + ring + ((lane & 15) >> 1) * PIECE + (lane & 1) * 512 + (((lane >> 4) ^ (lane & 1)) * 16);
    const lptr pw0 = smem + LDS_P + frag_wr_u(frag_wr(lane), w & 1, w >> 1);
    bf16x8 qf[8][5];
    float ref[5], l[5];
#define KDMA(i, dst, vo, srd, so, ps) do { SCHED_FENCE(); dma_piece<(i) & 3>((dst) + ((i) >> 2) * HALF, vo, srd, (so) + (i) * (ps) + ((i) >> 2) * HALF); SCHED_FENCE(); } while (0)
    {
      bf16x8 kf[8];
      WAIT_VM(9);
#pragma unroll
      for (int i = 0; i < 8; ++i) kf[i] = ld128(ka0 + 64 * i);
      WAIT_LGKM0(); BAR();
      STAMP(3);
      const int b0 = 16 * (lane ^ (2 * (lane >> 4)));
      f32x4 s[5];
#pragma unroll
      for (int t = 0; t < 5; ++t) s[t] = f32x4{0.f, 0.f, 0.f, 0.f};
#define QREAD(ks) do { _Pragma("unroll") for (int t = 0; t < 5; ++t) qf[ks][t] = ld128(smem + LDS_X + (3 - ((ks) >> 1)) * PBUF + (((ks) & 1) * 5 + t) * 1024 + (((ks) & 1) ? (b0 ^ 16) : b0)); } while (0)
#define QSTEP(ks) do { if ((ks) < 6) QREAD((ks) < 6 ? (ks) + 2 : 0); \
        _Pragma("unroll") for (int t = 0; t < 5; ++t) s[t] = mfma(kf[ks], qf[ks][t], s[t]); \
        MIX5(); KDMA(ks, ldsb + ring, kv, srdK, 128 + 2 * KVTILE, KPS); } while (0)
      QREAD(0); QREAD(1);
      SCHED_FENCE();
      QSTEP(0); QSTEP(1); QSTEP(2); QSTEP(3); QSTEP(4); QSTEP(5); QSTEP(6); QSTEP(7);
#undef QSTEP
#undef QREAD
      const lptr rb = smem + LDS_REF + (lane & 15) * 4;
#pragma unroll
      for (int t = 0; t < 5; ++t) *(LAS float*)(rb + w * 320 + t * 64) = fmaxf(NEG, ceilf(red_max_g(max4(s[t])) * SCL));
      WAIT_LGKM0(); BAR();
      STAMP(4);
      float rr[5][4];
      ld_refs(rr, rb);
#pragma unroll
      for (int t = 0; t < 5; ++t) {
        ref[t] = max2(max3(rr[t][0], rr[t][1], rr[t][2]), rr[t][3]);
        f32x4 p;
#pragma unroll
        for (int r = 0; r < 4; ++r) p[r] = ex2(fmaf(s[t][r], SCL, -ref[t]));
        l[t] = (p[0] + p[1]) + (p[2] + p[3]);
        *(LAS u32x2*)(pw0 + PBUF + t * 1024) = pk4(p);
      }
      WAIT_LGKM0(); BAR();
      STAMP(5);
    }
    auto adopt = [&](int par) {
      const u32x4 ch = *(LAS u32x4*)(smem + LDS_CHG + par * 16);
      if (UNLIKELY(__builtin_amdgcn_readfirstlane(ch[0] | ch[1] | ch[2] | ch[3]))) {
        float rr[5][4];
        ld_refs(rr, smem + LDS_REF + par * 1280 + (opaque(lane) & 15) * 4);
#pragma unroll
        for (int t = 0; t < 5; ++t) {
          float m = ref[t];
#pragma unroll
          for (int ww = 0; ww < 4; ++ww) m = fmaxf(m, ch[ww] ? rr[t][ww] : NEG);
          const float f = ex2(ref[t] - m);
          l[t] *= f; ref[t] = m;
          LAS u32x2* pp = (LAS u32x2*)(pw0 + (par ^ 1) * PBUF + t * 1024);
          const u32x2 pv = *pp;
          *pp = u32x2{pk2(blo(pv[0]) * f, bhi(pv[0]) * f), pk2(blo(pv[1]) * f, bhi(pv[1]) * f)};
          SCHED_FENCE();
        }
        WAIT_LGKM0(); BAR();
      }
    };
    rsrc_t ksrd = srdK; unsigned ksoff = 128 + 3 * KVTILE, kps = KPS, kss = KVTILE, kve = kv;
#pragma unroll 1
    for (int T = 1; T < NTILE; ++T) {
      if (UNLIKELY(T == NTILE - 2)) {
        STAMP(6);
        ksrd = srdWo; ksoff = 0; kps = WOP - PIECE; kss = WOSLOT; kve = row_src(opaque(lane), DM * 2);
      }
      WAIT_VM(8);
      const lptr ka = ka0 + (T & 1) * SLOT;
      bf16x8 kf[8];
#pragma unroll
      for (int i = 0; i < 8; ++i) kf[i] = ld128(ka + 64 * i);
      adopt((T + 1) & 1);
      WAIT_LGKM0();
      f32x4 s[5];
#pragma unroll
      for (int t = 0; t < 5; ++t) s[t] = f32x4{0.f, 0.f, 0.f, 0.f};
      const unsigned kdst = ldsb + ring + (T & 1) * SLOT;
#define KSTEP(i) do { _Pragma("unroll") for (int t = 0; t < 5; ++t) s[t] = mfma(kf[i], qf[i][t], s[t]); KDMA(i, kdst, kve, ksrd, ksoff, kps); } while (0)
      KSTEP(0); KSTEP(1); KSTEP(2); KSTEP(3); KSTEP(4); KSTEP(5); KSTEP(6); KSTEP(7);
#undef KSTEP
      ksoff += kss;
      float e4[5];
#pragma unroll
      for (int t = 0; t < 5; ++t) e4[t] = fmaf(max4(s[t]), SCL, -ref[t]);
      const float mx = max3(max3(e4[0], e4[1], e4[2]), e4[3], e4[4]);
      unsigned changed = 0;
      if (UNLIKELY(__builtin_amdgcn_ballot_w64(mx > THR))) {
        changed = 1;
        const int ln = opaque(lane);
        const lptr rb = smem + LDS_REF + ((T & 1) * 4 + w) * 320 + (ln & 15) * 4;
#pragma unroll
        for (int t = 0; t < 5; ++t) {
          const float m = red_max_g(max4(s[t]));
          const float nr = fmaxf(ref[t], ceilf(m * SCL));
          l[t] *= ex2(ref[t] - nr); ref[t] = nr;
          *(LAS float*)(rb + t * 64) = nr;
          SCHED_FENCE();
        }
      }
      const lptr pw = pw0 + ((T + 1) & 1) * PBUF;
#pragma unroll
      for (int t = 0; t < 5; ++t) {
        f32x4 p;
#pragma unroll
        for (int r = 0; r < 4; ++r) p[r] = ex2(fmaf(s[t][r], SCL, -ref[t]));
        l[t] += (p[0] + p[1]) + (p[2] + p[3]);
        *(LAS u32x2*)(pw + t * 1024) = pk4(p);
      }
      *(LAS unsigned*)(smem + LDS_CHG + ((T & 1) * 4 + w) * 4) = changed;
      WAIT_LGKM0(); BAR();
    }
#undef KDMA
    STAMP(7);
    adopt(1);
    const int ln = opaque(lane);
#pragma unroll
    for (int t = 0; t < 5; ++t) *(LAS float*)(smem + LDS_L + (w * 80 + t * 16 + (ln & 15)) * 4) = red_sum_g(l[t]);
  } else {
    const int lane = tid & 63;
    rsrc_t dsrd = mk_srd(wqh + v * 128, DM * WQROW - h * DH * 2 - v * 128);
    unsigned dsoff = 64 * WQROW, dps = 8 * WQROW - PIECE, dss = 64 * WQROW, dve = row_src(lane, WQROW);
    dma_half(ldsb + ring, dve, dsrd, 0, dps);
    dma_half(ldsb + ring + HALF, dve, dsrd, 32 * WQROW, dps);
    f32x4 acc[4][5];
    float mo[5];
    const int fsw = (lane >> 2) & 2;
    const lptr a01 = smem + ring + (lane >> 4) * PIECE + ((lane >> 2) & 3) * 128 + 8 * (lane & 3) + 32 * fsw, a23 = a01 + 64 - 64 * fsw;
    const lptr bx = smem + LDS_P + 16 * (lane ^ (2 * (lane >> 4))), by = smem + LDS_P + 5120 + ((16 * (lane ^ (2 * (lane >> 4)))) ^ 16);
    bf16x8 ya[4], yb[5];
#define AZERO() do { _Pragma("unroll") for (int u = 0; u < 4; ++u) _Pragma("unroll") for (int t = 0; t < 5; ++t) acc[u][t] = f32x4{0.f, 0.f, 0.f, 0.f}; \
                     _Pragma("unroll") for (int u = 0; u < 4; ++u) ya[u] = __builtin_bit_cast(bf16x8, u32x4{0, 0, 0, 0}); \
                     _Pragma("unroll") for (int t = 0; t < 5; ++t) yb[t] = __builtin_bit_cast(bf16x8, u32x4{0, 0, 0, 0}); } while (0)
    AZERO();
#pragma unroll
    for (int t = 0; t < 5; ++t) mo[t] = NEG;
    SCHED_FENCE();
    dma_half(ldsb + ring + SLOT, dve, dsrd, 64 * WQROW, dps);
    int nsw = 5, nfl = 6;
    unsigned chm = 0;
    auto step = [&](int n, auto par_) {
      constexpr int PAR = decltype(par_)::value;
      WAIT_LGKM0(); BAR();
      WAIT_VM(8);
      bf16x8 xa[4], xb_[5];
      u32x4 ch; unsigned rare;
      {
        const unsigned ddst = ldsb + ring + (1 - PAR) * SLOT + HALF, dso = dsoff + 4 * dps + HALF;
#define YMM(u) do { _Pragma("unroll") for (int t = 0; t < 5; ++t) acc[u][t] = mfma(ya[u], yb[t], acc[u][t]); } while (0)
#define YDMA(u) do { SCHED_FENCE(); dma_piece<u>(ddst, dve, dsrd, dso + (u) * dps); SCHED_FENCE(); } while (0)
        SCHED_FENCE();
        ch = *(LAS u32x4*)(smem + LDS_CHG + (1 - PAR) * 16); xa[0] = ldtr(a01 + PAR * SLOT); xa[1] = ldtr(a01 + PAR * SLOT + 32);
        YMM(0); MIX5(); YDMA(0);
#pragma unroll
        for (int t = 0; t < 5; ++t) xb_[t] = ld128(bx + PAR * PBUF + t * 1024);
        YMM(1); MIX5(); YDMA(1);
        xa[2] = ldtr(a23 + PAR * SLOT); xa[3] = ldtr(a23 + PAR * SLOT + 32);
        YMM(2); MIX5(); YDMA(2);
        rare = __builtin_amdgcn_readfirstlane(ch[0] | ch[1] | ch[2] | ch[3]) & chm;
        dsoff += dss;
        YMM(3); YDMA(3);
#undef YDMA
#undef YMM
      }
      if (PAR == 1 && UNLIKELY(n == nsw)) {
        if (n == 5) {
          dsrd = mk_srd(vbase + v * 128, wid == 7 ? 0 : SK * DH * 2 - v * 128);
          dsoff = 0; dps = 8 * DH * 2 - PIECE; dss = KVTILE; dve = row_src(opaque(lane), DH * 2);
          nsw = 21;
          STAMP(9);
        } else {
          dsrd = srdWo; dsoff = 0; dps = WOP - PIECE; dss = WOSLOT; dve = row_src(opaque(lane), DM * 2);
        }
      }
      if (UNLIKELY(rare)) {
        WAIT_LGKM0(); BAR();
#pragma unroll
        for (int t = 0; t < 5; ++t) xb_[t] = ld128(bx + PAR * PBUF + t * 1024);
        float rr[5][4];
        ld_refs(rr, smem + LDS_REF + (1 - PAR) * 1280 + (opaque(lane) & 15) * 4);
#pragma unroll
        for (int t = 0; t < 5; ++t) {
          float mn = mo[t];
#pragma unroll
          for (int ww = 0; ww < 4; ++ww) mn = fmaxf(mn, ch[ww] ? rr[t][ww] : NEG);
          const float fo = ex2(mo[t] - mn);
          mo[t] = mn;
#pragma unroll
          for (int u = 0; u < 4; ++u) acc[u][t] *= fo;
          SCHED_FENCE();
        }
      }
      WAIT_VM(8); WAIT_LGKM0();
      {
        const unsigned ddst = ldsb + ring + PAR * SLOT;
#define XMM(u) do { _Pragma("unroll") for (int t = 0; t < 5; ++t) acc[u][t] = mfma(xa[u], xb_[t], acc[u][t]); } while (0)
#define XDMA(u) do { SCHED_FENCE(); dma_piece<u>(ddst, dve, dsrd, dsoff + (u) * dps); SCHED_FENCE(); } while (0)
        SCHED_FENCE();
        ya[0] = ldtr(a01 + PAR * SLOT + HALF); ya[1] = ldtr(a01 + PAR * SLOT + HALF + 32);
        XMM(0); MIX5(); XDMA(0);
#pragma unroll
        for (int t = 0; t < 5; ++t) yb[t] = ld128(by + PAR * PBUF + t * 1024);
        XMM(1); MIX5(); XDMA(1);
        ya[2] = ldtr(a23 + PAR * SLOT + HALF); ya[3] = ldtr(a23 + PAR * SLOT + HALF + 32);
        XMM(2); MIX5(); XDMA(2);
        XMM(3); XDMA(3);
#undef XDMA
#undef XMM
      }
    };
#pragma unroll 1
    for (int n = 0;; n += 2) {
      step(n, IC<0>{});
      if (UNLIKELY(n == nfl)) {
        if (n == 22) STAMP(7);
        if (n == 6) STAMP(2);
        if (wid == 7) WAIT_VM(0);
        WAIT_LGKM0(); BAR();
        const int ln = opaque(lane);
        float lr[5][4];
        ld_refs(lr, smem + LDS_L + (ln & 15) * 4);
        const int a0 = frag_wr(ln);
        const lptr xo = smem + LDS_X + (3 - v) * PBUF;
        float inv[5];
#pragma unroll
        for (int t = 0; t < 5; ++t) {
          const float ls = (lr[t][0] + lr[t][1]) + (lr[t][2] + lr[t][3]);
          const float r = __builtin_amdgcn_rcpf(ls);
          inv[t] = n == 22 ? r * (2.0f - ls * r) : 1.0f;
        }
#pragma unroll
        for (int u = 0; u < 4; ++u)
#pragma unroll
          for (int t = 0; t < 5; ++t) *(LAS u32x2*)(xo + frag_wr_u(a0, u & 1, u >> 1) + t * 1024) = pk4(mfma(ya[u], yb[t], acc[u][t]) * inv[t]);
        if (n == 6) STAMP(3);
        WAIT_LGKM0(); BAR();
        if (n == 22) break;
        AZERO();
        BAR();
        STAMP(4);
        if (wid == 7) {
          dsrd = mk_srd(vbase + 3 * 128, SK * DH * 2 - 3 * 128);
          dma_half(ldsb + ring + SLOT, dve, dsrd, 0, dps);
          dma_half(ldsb + ring + SLOT + HALF, dve, dsrd, KVTILE / 2, dps);
          dma_half(ldsb + ring, dve, dsrd, KVTILE, dps);
        }
        float rr[5][4];
        ld_refs(rr, smem + LDS_REF + (ln & 15) * 4);
#pragma unroll
        for (int t = 0; t < 5; ++t) mo[t] = max2(max3(rr[t][0], rr[t][1], rr[t][2]), rr[t][3]);
        nfl = 22; chm = ~0u;
      }
      step(n + 1, IC<1>{});
    }
  }
  if (wid < 7) {
    const int lane = opaque(tid) & 63;
    const int fsw = (lane >> 2) & 2, trb = (lane >> 4) * PIECE + ((lane >> 2) & 3) * 128 + 8 * (lane & 3);
    const bool qk = wid < 4;
    const unsigned c0 = qk ? ring : ring + SLOT, c1 = qk ? ring + SLOT : ring, c2 = qk ? ring : ring - 4 * RING, c3 = c2 + SLOT;
    const unsigned we = row_src(lane, DM * 2);
    constexpr unsigned WPS = WOP - PIECE;
    bf16x8 wf[8][4];
#define WREAD(s_, slot) do { _Pragma("unroll") for (int ks = 0; ks < 2; ++ks) _Pragma("unroll") for (int u = 0; u < 4; ++u) \
      wf[2 * (s_) + ks][u] = ldtr(smem + (slot) + trb + ks * HALF + 32 * (u ^ fsw)); } while (0)
#define WDMA(s_, slot) do { dma_half(ldsb + (slot), we, srdWo, (s_) * WOSLOT, WPS); dma_half(ldsb + (slot) + HALF, we, srdWo, (s_) * WOSLOT + 4 * WOP, WPS); } while (0)
    if (!qk) { dma_half(ldsb + c1 + HALF, we, srdWo, WOSLOT + 4 * WOP, WPS); WDMA(2, c2); WDMA(3, c3); WAIT_VM(24); } else WAIT_VM(8);
    WREAD(0, c0);
    if (qk) { WAIT_LGKM0(); WDMA(2, c2); WAIT_VM(8); } else WAIT_VM(16);
    WREAD(1, c1);
    if (qk) { WAIT_LGKM0(); WDMA(3, c3); BAR(); }
    WAIT_VM(8);
    WREAD(2, c2);
    WAIT_VM(0);
    WREAD(3, c3);
#undef WDMA
#undef WREAD
    if (qk) { WAIT_LGKM0(); BAR(); }
    STAMP(8);
    const rsrc_t srdP = mk_srd(ws + WS_PART + ((size_t)(grp * NBLK + wid * 10) * HQ + h) * 1024, 9 * 16384 + 1024);
    const int b0 = 16 * (lane ^ (2 * (lane >> 4)));
    bf16x8 of[2][8];
#define OREAD(t) do { _Pragma("unroll") for (int ks = 0; ks < 8; ++ks) of[(t) & 1][ks] = ld128(smem + LDS_X + (3 - (ks >> 1)) * PBUF + ((ks & 1) * 5 + (t)) * 1024 + ((ks & 1) ? (b0 ^ 16) : b0)); } while (0)
    OREAD(0);
#pragma unroll
    for (int t = 0; t < 5; ++t) {
      if (t < 4) OREAD(t < 4 ? t + 1 : 0);
      f32x4 o[4];
#pragma unroll
      for (int u = 0; u < 4; ++u) o[u] = f32x4{0.f, 0.f, 0.f, 0.f};
#pragma unroll
      for (int ks = 0; ks < 8; ++ks)
#pragma unroll
        for (int u = 0; u < 4; ++u) o[u] = mfma(wf[ks][u], of[t & 1][ks], o[u]);
#pragma unroll
      for (int m = 0; m < 2; ++m) {
        const u32x2 a = pk4(o[2 * m]), c = pk4(o[2 * m + 1]);
        __builtin_amdgcn_raw_buffer_store_b128(u32x4{a[0], a[1], c[0], c[1]}, srdP, lane * 16 + (t * 2 + m) * 16384, 0, 16);
      }
      SCHED_FENCE();
    }
#undef OREAD
  }
  STAMP(10);
  {
    const int lane = opaque(tid) & 63;
    gu64* const ctl = (gu64*)(ws + grp * 4096);
    const unsigned long long tag = *(LAS unsigned long long*)(smem + LDS_EP) + 1;
    if (wid < 7) {
      WAIT_VM(0);
      STAMP(11);
      if (lane == 0) __hip_atomic_store(ctl + 16 * (1 + wid) + h, tag, __ATOMIC_RELAXED, __HIP_MEMORY_SCOPE_AGENT);
    }
    const int blk = wid < 3 ? h + 16 * wid : wid == 7 ? 40 + h : 56 + h;
    if (wid < 3 ? blk < 40 : (wid == 7 || (wid == 3 && blk < NBLK))) {
      const int cb = blk / 10;
      const rsrc_t srd = mk_srd(ws + WS_PART + (size_t)(grp * NBLK + blk) * 16384, 16384);
      const gu64* fa = ctl + 16 * (1 + cb) + (lane & 15);
      const unsigned v0 = lane * 16, v1 = v0 + 4096, v2 = v0 + 8192, v3 = v0 + 12288;
      u32x4 pv[16]; unsigned long long fl; unsigned pend, nw, spin;
#define FETCH(i, vo, off) "s_bitcmp1_b32 %[nw], " #i "\n\ts_cselect_b64 exec, -1, 0\n\tbuffer_load_dwordx4 %" #i ", %[" #vo "], %[srd], 0 offen offset:" #off " sc1\n\t"
      asm volatile("s_mov_b32 %[pend], 0xffff\n\ts_mov_b32 %[spin], 0x40000\n"
                   "1:\n\tglobal_load_dwordx2 %[fl], %[fa], off sc1\n\ts_waitcnt vmcnt(0)\n\tv_cmp_eq_u64 vcc, %[fl], %[tag]\n\ts_nop 0\n\t"
                   "s_and_b32 %[nw], vcc_lo, %[pend]\n\ts_cbranch_scc0 2f\n\t"
                   FETCH(0, v0, 0) FETCH(1, v0, 1024) FETCH(2, v0, 2048) FETCH(3, v0, 3072) FETCH(4, v1, 0) FETCH(5, v1, 1024) FETCH(6, v1, 2048) FETCH(7, v1, 3072)
                   FETCH(8, v2, 0) FETCH(9, v2, 1024) FETCH(10, v2, 2048) FETCH(11, v2, 3072) FETCH(12, v3, 0) FETCH(13, v3, 1024) FETCH(14, v3, 2048) FETCH(15, v3, 3072)
                   "s_mov_b64 exec, -1\n\ts_andn2_b32 %[pend], %[pend], %[nw]\n\ts_cbranch_scc0 3f\n"
                   "2:\n\ts_sleep 1\n\ts_sub_u32 %[spin], %[spin], 1\n\ts_cbranch_scc0 1b\n"
                   "3:\n\ts_waitcnt vmcnt(0)"
                   : "=&v"(pv[0]), "=&v"(pv[1]), "=&v"(pv[2]), "=&v"(pv[3]), "=&v"(pv[4]), "=&v"(pv[5]), "=&v"(pv[6]), "=&v"(pv[7]),
                     "=&v"(pv[8]), "=&v"(pv[9]), "=&v"(pv[10]), "=&v"(pv[11]), "=&v"(pv[12]), "=&v"(pv[13]), "=&v"(pv[14]), "=&v"(pv[15]),
                     [fl] "=&v"(fl), [pend] "=&s"(pend), [nw] "=&s"(nw), [spin] "=&s"(spin)
                   : [fa] "v"(fa), [tag] "v"(tag), [srd] "s"(srd), [v0] "v"(v0), [v1] "v"(v1), [v2] "v"(v2), [v3] "v"(v3)
                   : "memory", "vcc", "scc");
#undef FETCH
      STAMP(12);
      if (h == 0 && wid == 0 && lane == 0) __hip_atomic_store(ctl, tag, __ATOMIC_RELAXED, __HIP_MEMORY_SCOPE_AGENT);
      float s[8];
#pragma unroll
      for (int k = 0; k < 8; ++k) s[k] = 0.f;
#pragma unroll
      for (int i = 0; i < 16; ++i)
#pragma unroll
        for (int k = 0; k < 4; ++k) { s[2 * k] += blo(pv[i][k]); s[2 * k + 1] += bhi(pv[i][k]); }
      const int t = (blk % 10) >> 1, m = blk & 1;
      char* dst = out + ((size_t)(b * SQ + t * 16 + (lane & 15)) * (2 * DM) + br * DM + cb * 64 + m * 32 + (lane >> 4) * 4) * 2;
      *(u32x2*)dst = u32x2{pk2(s[0], s[1]), pk2(s[2], s[3])};
      *(u32x2*)(dst + 32) = u32x2{pk2(s[4], s[5]), pk2(s[6], s[7])};
      STAMP(13);
    }
  }
  STAMP_FLUSH();
  asm volatile(".Lgqa_end:");
}

extern "C" void kernel_launch(void* const* d_in, const int* in_sizes, int n_in, void* d_out, int out_size, void* d_ws, size_t ws_size,
                              hipStream_t stream) {
  (void)in_sizes; (void)n_in; (void)out_size; (void)ws_size;
  gqa_main<<<256, 512, 0, stream>>>((const char*)d_in[0], (const char*)d_in[1], (const char*)d_in[2], (const char*)d_in[3], (const char*)d_in[4],
                                    (const char*)d_in[5], (const char*)d_in[6], (const char*)d_in[7], (const char*)d_in[8], (const char*)d_in[9],
                                    (char*)d_ws, (char*)d_out);
}
```

```cpp
#include <hip/hip_runtime.h>
#include <hip/hip_bf16.h>
#include <stdint.h>

typedef __bf16 bf16x8 __attribute__((ext_vector_type(8)));
typedef __bf16 bf16x2 __attribute__((ext_vector_type(2)));
typedef short s16x4 __attribute__((ext_vector_type(4)));
typedef float f32x4 __attribute__((ext_vector_type(4)));
typedef float f32x2 __attribute__((ext_vector_type(2)));
typedef unsigned u32x4 __attribute__((ext_vector_type(4)));
typedef unsigned u32x2 __attribute__((ext_vector_type(2)));
#define LAS __attribute__((address_space(3)))
#define GAS __attribute__((address_space(1)))
typedef LAS char* lptr;
typedef GAS unsigned long long gu64;
typedef __amdgpu_buffer_rsrc_t rsrc_t;

constexpr int NB = 8, SQ = 80, DM = 448, HQ = 16, HKV = 8, SK = 1024, DH = 256;
constexpr int NTILE = SK / 64;
constexpr int PIECE = 1056, HALF = 4 * PIECE, SLOT = 8 * PIECE, RING = 2 * SLOT;
constexpr int LDS_X = 7 * RING;
constexpr int LDS_P = LDS_X + 20480, PBUF = 10240;
constexpr int LDS_REF = LDS_X + 40960;
constexpr int LDS_CHG = LDS_REF + 2560;
constexpr int LDS_L = LDS_CHG + 32;
constexpr int LDS_EP = LDS_L + 1280;
constexpr int LDS_SCR = LDS_EP + 64;
constexpr int LDS_TOTAL = 163840;
static_assert(LDS_SCR + 256 <= LDS_TOTAL && LDS_EP % 16 == 0 && RING <= 20480, "lds");

constexpr float SCL = 0.0625f * 1.4426950408889634f;
constexpr float THR = 10.f;
constexpr float NEG = -1.0e30f;
constexpr int NBLK = 70;
constexpr size_t WS_PART = 1 << 20;
constexpr unsigned OOB = 0x7ffffff0u;

#define WAIT_VM(n) asm volatile("s_waitcnt vmcnt(" #n ")" ::: "memory")
#define WAIT_LGKM0() do { asm volatile("" ::: "memory"); __builtin_amdgcn_s_waitcnt(0xC07F); asm volatile("" ::: "memory"); } while (0)
#define UNLIKELY(x) __builtin_expect(!!(x), 0)
#define LIKELY(x) __builtin_expect(!!(x), 1)
#define SCHED_FENCE() __builtin_amdgcn_sched_barrier(0)
#define MIX5() do { _Pragma("unroll") for (int i_ = 0; i_ < 5; ++i_) { __builtin_amdgcn_sched_group_barrier(0x8, 1, 0); __builtin_amdgcn_sched_group_barrier(0x100, 1, 0); } } while (0)
#define BAR() do { SCHED_FENCE(); asm volatile("" ::: "memory"); __builtin_amdgcn_s_barrier(); asm volatile("" ::: "memory"); SCHED_FENCE(); } while (0)

#define FSTAMP(i) do {} while (0)
#define STAMP(i) do {} while (0)
#define STAMP_DECL do {} while (0)
#define STAMP_FLUSH() do {} while (0)

#define DMA_POL "nt"
template <int P>
__device__ __forceinline__ void dma_piece(unsigned m0v, unsigned voff, rsrc_t srd, unsigned soff) {
  if (P == 0) asm volatile("s_mov_b32 m0, %0\n\ts_nop 0\n\tbuffer_load_dwordx4 %1, %2, %3 offen " DMA_POL " lds" :: "s"(m0v), "v"(voff), "s"(srd), "s"(soff) : "memory");
  else asm volatile("buffer_load_dwordx4 %0, %1, %2 offen offset:%3 " DMA_POL " lds" :: "v"(voff), "s"(srd), "s"(soff), "i"(P * PIECE) : "memory");
}
__device__ __forceinline__ void dma_half(unsigned m0v, unsigned voff, rsrc_t srd, unsigned soff, unsigned ps) {
  dma_piece<0>(m0v, voff, srd, soff); dma_piece<1>(m0v, voff, srd, soff + ps); dma_piece<2>(m0v, voff, srd, soff + 2 * ps); dma_piece<3>(m0v, voff, srd, soff + 3 * ps);
}
__device__ __forceinline__ void touch(unsigned m0v, unsigned voff, rsrc_t srd) {
  asm volatile("s_mov_b32 m0, %0\n\ts_nop 0\n\tbuffer_load_dword %1, %2, 0 offen lds" :: "s"(m0v), "v"(voff), "s"(srd) : "memory");
}
__device__ __forceinline__ bf16x8 ld128(lptr p) { return *(LAS bf16x8*)p; }
__device__ __forceinline__ bf16x8 ldtr(lptr p) {
  s16x4 a = __builtin_amdgcn_ds_read_tr16_b64_v4i16((LAS s16x4*)p);
  s16x4 b = __builtin_amdgcn_ds_read_tr16_b64_v4i16((LAS s16x4*)(p + 512));
  return __builtin_bit_cast(bf16x8, __builtin_shufflevector(a, b, 0, 1, 2, 3, 4, 5, 6, 7));
}
__device__ __forceinline__ unsigned pk2(float a, float b) { f32x2 t = {a, b}; return __builtin_bit_cast(unsigned, __builtin_convertvector(t, bf16x2)); }
__device__ __forceinline__ u32x2 pk4(f32x4 v) { u32x2 r = {pk2(v[0], v[1]), pk2(v[2], v[3])}; return r; }
__device__ __forceinline__ float blo(unsigned u) { return __uint_as_float(u << 16); }
__device__ __forceinline__ float bhi(unsigned u) { return __uint_as_float(u & 0xffff0000u); }
__device__ __forceinline__ f32x4 mfma(bf16x8 a, bf16x8 b, f32x4 c) { return __builtin_amdgcn_mfma_f32_16x16x32_bf16(a, b, c, 0, 0, 0); }
__device__ __forceinline__ void mfma_ip(f32x4& c, bf16x8 a, bf16x8 b) { asm volatile("v_mfma_f32_16x16x32_bf16 %0, %1, %2, %0" : "+v"(c) : "v"(a), "v"(b)); }
#define MFMA_SETTLE() do { SCHED_FENCE(); asm volatile("s_nop 15" ::: "memory"); SCHED_FENCE(); } while (0)
__device__ __forceinline__ float ex2(float x) { return __builtin_amdgcn_exp2f(x); }
__device__ __forceinline__ float max2(float a, float b) { return __builtin_elementwise_maximum(a, b); }
__device__ __forceinline__ float max3(float a, float b, float c) { return max2(max2(a, b), c); }
__device__ __forceinline__ float max4(f32x4 v) { return max2(max3(v[0], v[1], v[2]), v[3]); }
__device__ __forceinline__ float red_max_g(float v) {
  auto a = __builtin_amdgcn_permlane16_swap(__float_as_uint(v), __float_as_uint(v), false, false);
  v = max2(__uint_as_float(a[0]), __uint_as_float(a[1]));
  auto b = __builtin_amdgcn_permlane32_swap(__float_as_uint(v), __float_as_uint(v), false, false);
  return max2(__uint_as_float(b[0]), __uint_as_float(b[1]));
}
__device__ __forceinline__ float red_sum_g(float v) {
  auto a = __builtin_amdgcn_permlane16_swap(__float_as_uint(v), __float_as_uint(v), false, false);
  v = __uint_as_float(a[0]) + __uint_as_float(a[1]);
  auto b = __builtin_amdgcn_permlane32_swap(__float_as_uint(v), __float_as_uint(v), false, false);
  return __uint_as_float(b[0]) + __uint_as_float(b[1]);
}
template <int V> struct IC { static constexpr int value = V; };
__device__ __forceinline__ int opaque(int v) { asm volatile("" : "+v"(v)); return v; }
__device__ __forceinline__ rsrc_t mk_srd(const void* p, unsigned bytes) { return __builtin_amdgcn_make_buffer_rsrc((void*)p, (short)0, (int)bytes, 0x00020000); }
__device__ __forceinline__ unsigned row_src(int lane, int stride) { const int srow = lane >> 3; return srow * stride + (((lane & 7) ^ (((srow >> 1) & 1) << 2)) * 16); }
__device__ __forceinline__ void ld_refs(float (*rr)[4], lptr rb) {
#pragma unroll
  for (int t = 0; t < 5; ++t)
#pragma unroll
    for (int ww = 0; ww < 4; ++ww) rr[t][ww] = *(LAS float*)(rb + ww * 320 + t * 64);
#pragma unroll
  for (int t = 0; t < 5; ++t) asm volatile("" : "+v"(rr[t][0]), "+v"(rr[t][1]), "+v"(rr[t][2]), "+v"(rr[t][3]));
}
__device__ __forceinline__ int frag_wr(int lane) { return 16 * ((lane & 15) ^ (2 * (lane >> 5))) + 256 * (lane >> 5) + 8 * ((lane >> 4) & 1); }
__device__ __forceinline__ int frag_wr_u(int a0, int ulo, int uhi) { return (a0 ^ (16 * (4 * ulo + uhi))) + 512 * ulo + 5120 * uhi; }

__global__ __launch_bounds__(512, 2)
void gqa_main(const char* __restrict__ xA, const char* __restrict__ kA, const char* __restrict__ vA, const char* __restrict__ wqA, const char* __restrict__ woA,
              const char* __restrict__ xB, const char* __restrict__ kB, const char* __restrict__ vB, const char* __restrict__ wqB, const char* __restrict__ woB,
              char* ws, char* out) {
  __shared__ __attribute__((aligned(16))) char smem_raw[LDS_TOTAL];
  asm volatile(".Lgqa_beg:");
  const lptr smem = (lptr)smem_raw;
  const unsigned ldsb = (unsigned)(uintptr_t)smem;
  const int tid = threadIdx.x;
  const int wid = __builtin_amdgcn_readfirstlane(tid >> 6);
  const int bid = blockIdx.x, xcd = bid & 7, jj = bid >> 3;
  const int br = xcd >> 2, b = jj >> 2, kvh = 2 * (xcd & 3) + ((jj >> 1) & 1), h = 2 * kvh + (jj & 1);
  const int grp = br * NB + b;
  const char* const xb = (br ? xB : xA) + (size_t)b * SQ * DM * 2;
  const char* const wqh = (br ? wqB : wqA) + h * DH * 2;
  const char* const woh = (br ? woB : woA) + (size_t)h * DH * DM * 2;
  const char* const vbase = (br ? vB : vA) + (size_t)(b * HKV + kvh) * SK * DH * 2;
  const rsrc_t srdWo = mk_srd(woh + wid * 128, wid < 7 ? DH * DM * 2 - wid * 128 : 0);
  constexpr unsigned WOSLOT = 64 * DM * 2, WOP = 8 * DM * 2, WQROW = HQ * DH * 2, KVTILE = 64 * DH * 2;
  const unsigned ring = wid * RING;
#define ROT 0
  const unsigned rot = ROT ? ((b * HKV + kvh + 5 * br) * 7 & 15) * KVTILE : 0u;
  constexpr unsigned KVMASK = 16 * KVTILE - 1;
  STAMP_DECL;
  STAMP(0);

  const int v = wid - 4;

  if (wid < 4) {
    const int w = wid;
    const int lane = tid & 63;
    __builtin_amdgcn_s_setprio(1);
    const rsrc_t srdK = mk_srd((br ? kB : kA) + ((size_t)(b * HKV + kvh) * SK + w * 16) * (DH * 2) - 128, (SK - w * 16) * DH * 2 + 128);
    const unsigned kv = (lane >> 5) * 512 + (((lane & 31) ^ (lane >> 5)) * 16);
    constexpr unsigned KPS = 1024 - PIECE;
#define ABL_K srdK
    {
      u32x4 xr[7][3];
      const rsrc_t srdX = mk_srd(xb, SQ * DM * 2);
      const unsigned xo = (8 * w + (lane >> 3)) * (DM * 2) + (lane & 7) * 16;
#define XLOAD(j) do { _Pragma("unroll") for (int i = 0; i < 3; ++i) \
        asm volatile("buffer_load_dwordx4 %0, %1, %2, %3 offen offset:%4" : "=v"(xr[j][i]) : "v"(xo), "s"(srdX), "s"(i * 32 * DM * 2), "i"((j) * 128) : "memory"); } while (0)
#define XWAIT(j, n) asm volatile("s_waitcnt vmcnt(" #n ")" : "+v"(xr[j][0]), "+v"(xr[j][1]), "+v"(xr[j][2]) :: "memory")
#define XWRITE(j) do { *(LAS u32x4*)(xdst + ((j) & 1) * PBUF) = xr[j][0]; *(LAS u32x4*)(xdst + ((j) & 1) * PBUF + 2048) = xr[j][1]; \
                       if (w < 2) *(LAS u32x4*)(xdst + ((j) & 1) * PBUF + 4096) = xr[j][2]; } while (0)
#define KHALF(n) dma_half(ldsb + ring + (n) * HALF, kv, ABL_K, 128 + ((rot + ((n) >> 1) * KVTILE) & KVMASK) + ((n) & 1) * 4096, KPS)
      XLOAD(0); XLOAD(1);
      if (lane < 8) *(LAS unsigned*)(smem + LDS_CHG + lane * 4) = 0;
      const int xg = lane & 3, xk = (lane >> 2) & 1;
      const lptr xdst = smem + LDS_P + (xk * 5 + (w >> 1)) * 1024 + 16 * ((8 * (w & 1) + (lane >> 3) + 16 * xg) ^ (2 * xg + xk));
      XWAIT(0, 3); XWRITE(0);
      XWAIT(1, 0); XWRITE(1);
      STAMP(1);
      WAIT_LGKM0(); BAR();
      STAMP(2);
      XLOAD(2); XLOAD(3); XLOAD(4); XLOAD(5); XLOAD(6);
      {
        const int id = 512 + 8 * (4 * lane + w) + b;
        touch(ldsb + LDS_SCR, lane < 40 ? (unsigned)((id >> 2) * WQROW + (id & 3) * 128) : OOB, mk_srd(wqh, DM * WQROW - h * DH * 2));
      }
#define NPRE 0
      const rsrc_t srdKt = mk_srd((br ? kB : kA) + (size_t)(b * HKV + kvh) * SK * DH * 2, NPRE > 2 ? SK * DH * 2 : 0), srdVt = mk_srd(vbase, NPRE > 0 ? SK * DH * 2 : 0);
      const unsigned to = lane < 32 ? (unsigned)(((jj & 1) * 128 + w * 32 + lane) * 128) : OOB;
#define PRE(i, srd, tile) do { if (NPRE > (i)) touch(ldsb + LDS_SCR, to + (tile) * KVTILE, srd); else touch(ldsb + LDS_SCR, OOB, srd); } while (0)
      BAR(); FSTAMP(3); XWAIT(2, 13); XWRITE(2); KHALF(0); PRE(0, srdVt, 0); WAIT_LGKM0();
      BAR(); FSTAMP(4); XWAIT(3, 15); XWRITE(3); KHALF(1); PRE(1, srdVt, 1); WAIT_LGKM0();
      BAR(); FSTAMP(5); XWAIT(4, 17); XWRITE(4); KHALF(2); PRE(2, srdKt, 2); WAIT_LGKM0();
      BAR(); FSTAMP(6); XWAIT(5, 19); XWRITE(5); KHALF(3); PRE(3, srdVt, 2); WAIT_LGKM0();
      BAR(); FSTAMP(7); XWAIT(6, 21); XWRITE(6); PRE(4, srdKt, 3); WAIT_LGKM0();
      BAR(); FSTAMP(8); PRE(5, srdVt, 3);
#undef PRE
      touch(ldsb + LDS_SCR, lane < 56 ? (unsigned)((8 * (4 * lane + w) + b) * 128) : OOB, mk_srd(woh, DH * DM * 2));
      if (w == 3) {
        const char* a = lane == 0 ? ws + WS_PART + (size_t)grp * NBLK * 16384 : lane == 1 ? ws + WS_PART + (size_t)(grp + 1) * NBLK * 16384 - 128 : out + (size_t)b * SQ * 4 * DM;
        if (lane < 3) asm volatile("s_mov_b32 m0, %0\n\ts_nop 0\n\tglobal_load_lds_dword %1, off" :: "s"(ldsb + LDS_SCR), "v"(a) : "memory");
      }
      BAR(); FSTAMP(9);
#undef XLOAD
#undef XWAIT
#undef XWRITE
#undef KHALF
    }
    const lptr ka0 = smem + ring + ((lane & 15) >> 1) * PIECE + (lane & 1) * 512 + (((lane >> 4) ^ (lane & 1)) * 16);
    const lptr pw0 = smem + LDS_P + frag_wr_u(frag_wr(lane), w & 1, w >> 1);
    bf16x8 qf[8][5];
    float ref[5], l[5];
#define KDMA(i, dst, vo, srd, so, ps) do { SCHED_FENCE(); dma_piece<(i) & 3>((dst) + ((i) >> 2) * HALF, vo, srd, (so) + (i) * (ps) + ((i) >> 2) * HALF); SCHED_FENCE(); } while (0)
    {
      bf16x8 kf[8];
      WAIT_VM(13);
#pragma unroll
      for (int i = 0; i < 8; ++i) kf[i] = ld128(ka0 + 64 * i);
      WAIT_LGKM0(); BAR();
      STAMP(3);
      const int b0 = 16 * (lane ^ (2 * (lane >> 4)));
      f32x4 s[5];
#pragma unroll
      for (int t = 0; t < 5; ++t) s[t] = f32x4{0.f, 0.f, 0.f, 0.f};
#define QREAD(ks) do { _Pragma("unroll") for (int t = 0; t < 5; ++t) qf[ks][t] = ld128(smem + LDS_X + (3 - ((ks) >> 1)) * PBUF + (((ks) & 1) * 5 + t) * 1024 + (((ks) & 1) ? (b0 ^ 16) : b0)); } while (0)
#define QSTEP(ks) do { if ((ks) < 6) QREAD((ks) < 6 ? (ks) + 2 : 0); \
        _Pragma("unroll") for (int t = 0; t < 5; ++t) s[t] = mfma(kf[ks], qf[ks][t], s[t]); \
        MIX5(); KDMA(ks, ldsb + ring, kv, srdK, 128 + ((rot + 2 * KVTILE) & KVMASK), KPS); } while (0)
      QREAD(0); QREAD(1);
      SCHED_FENCE();
      QSTEP(0); QSTEP(1); QSTEP(2); QSTEP(3); QSTEP(4); QSTEP(5); QSTEP(6); QSTEP(7);
#undef QSTEP
#undef QREAD
      const lptr rb = smem + LDS_REF + (lane & 15) * 4;
#pragma unroll
      for (int t = 0; t < 5; ++t) *(LAS float*)(rb + w * 320 + t * 64) = fmaxf(NEG, ceilf(red_max_g(max4(s[t])) * SCL));
      WAIT_LGKM0(); BAR();
      STAMP(4);
      float rr[5][4];
      ld_refs(rr, rb);
#pragma unroll
      for (int t = 0; t < 5; ++t) {
        ref[t] = max2(max3(rr[t][0], rr[t][1], rr[t][2]), rr[t][3]);
        f32x4 p;
#pragma unroll
        for (int r = 0; r < 4; ++r) p[r] = ex2(fmaf(s[t][r], SCL, -ref[t]));
        l[t] = (p[0] + p[1]) + (p[2] + p[3]);
        *(LAS u32x2*)(pw0 + PBUF + t * 1024) = pk4(p);
      }
      WAIT_LGKM0(); BAR();
      STAMP(5);
    }
    auto adopt = [&](int par) {
      const u32x4 ch = *(LAS u32x4*)(smem + LDS_CHG + par * 16);
      if (UNLIKELY(__builtin_amdgcn_readfirstlane(ch[0] | ch[1] | ch[2] | ch[3]))) {
        float rr[5][4];
        ld_refs(rr, smem + LDS_REF + par * 1280 + (opaque(lane) & 15) * 4);
#pragma unroll
        for (int t = 0; t < 5; ++t) {
          float m = ref[t];
#pragma unroll
          for (int ww = 0; ww < 4; ++ww) m = fmaxf(m, ch[ww] ? rr[t][ww] : NEG);
          const float f = ex2(ref[t] - m);
          l[t] *= f; ref[t] = m;
          LAS u32x2* pp = (LAS u32x2*)(pw0 + (par ^ 1) * PBUF + t * 1024);
          const u32x2 pv = *pp;
          *pp = u32x2{pk2(blo(pv[0]) * f, bhi(pv[0]) * f), pk2(blo(pv[1]) * f, bhi(pv[1]) * f)};
          SCHED_FENCE();
        }
        WAIT_LGKM0(); BAR();
      }
    };
    rsrc_t ksrd = srdK; unsigned ksoff = (rot + 3 * KVTILE) & KVMASK, kbias = 128, kps = KPS, kss = KVTILE, kve = kv;
#pragma unroll 1
    for (int T = 1; T < NTILE; ++T) {
      if (UNLIKELY(T == NTILE - 2)) {
        STAMP(6);
        ksrd = srdWo; ksoff = 0; kbias = 0; kps = WOP - PIECE; kss = WOSLOT; kve = row_src(opaque(lane), DM * 2);
      }
      WAIT_VM(8);
      const lptr ka = ka0 + (T & 1) * SLOT;
      bf16x8 kf[8];
#pragma unroll
      for (int i = 0; i < 8; ++i) kf[i] = ld128(ka + 64 * i);
      adopt((T + 1) & 1);
      WAIT_LGKM0();
      f32x4 s[5];
#pragma unroll
      for (int t = 0; t < 5; ++t) s[t] = f32x4{0.f, 0.f, 0.f, 0.f};
      const unsigned kdst = ldsb + ring + (T & 1) * SLOT;
#define KSTEP(i) do { _Pragma("unroll") for (int t = 0; t < 5; ++t) s[t] = mfma(kf[i], qf[i][t], s[t]); KDMA(i, kdst, kve, ksrd, ksoff + kbias, kps); } while (0)
      KSTEP(0); KSTEP(1); KSTEP(2); KSTEP(3); KSTEP(4); KSTEP(5); KSTEP(6); KSTEP(7);
#undef KSTEP
      ksoff = (ksoff + kss) & KVMASK;
      float e4[5];
#pragma unroll
      for (int t = 0; t < 5; ++t) e4[t] = fmaf(max4(s[t]), SCL, -ref[t]);
      const float mx = max3(max3(e4[0], e4[1], e4[2]), e4[3], e4[4]);
      unsigned changed = 0;
      if (UNLIKELY(__builtin_amdgcn_ballot_w64(mx > THR))) {
        changed = 1;
        const int ln = opaque(lane);
        const lptr rb = smem + LDS_REF + ((T & 1) * 4 + w) * 320 + (ln & 15) * 4;
#pragma unroll
        for (int t = 0; t < 5; ++t) {
          const float m = red_max_g(max4(s[t]));
          const float nr = fmaxf(ref[t], ceilf(m * SCL));
          l[t] *= ex2(ref[t] - nr); ref[t] = nr;
          *(LAS float*)(rb + t * 64) = nr;
          SCHED_FENCE();
        }
      }
      const lptr pw = pw0 + ((T + 1) & 1) * PBUF;
#pragma unroll
      for (int t = 0; t < 5; ++t) {
        f32x4 p;
#pragma unroll
        for (int r = 0; r < 4; ++r) p[r] = ex2(fmaf(s[t][r], SCL, -ref[t]));
        l[t] += (p[0] + p[1]) + (p[2] + p[3]);
        *(LAS u32x2*)(pw + t * 1024) = pk4(p);
      }
      *(LAS unsigned*)(smem + LDS_CHG + ((T & 1) * 4 + w) * 4) = changed;
      WAIT_LGKM0(); BAR();
    }
#undef KDMA
    STAMP(7);
    adopt(1);
    const int ln = opaque(lane);
#pragma unroll
    for (int t = 0; t < 5; ++t) *(LAS float*)(smem + LDS_L + (w * 80 + t * 16 + (ln & 15)) * 4) = red_sum_g(l[t]);
  } else {
    const int lane = tid & 63;
    if (wid == 7) {
      unsigned long long pc; unsigned cb_, ce_;
      asm volatile("s_getpc_b64 %0\n.Lgqa_pc:\n\ts_mov_b32 %1, .Lgqa_pc-.Lgqa_beg\n\ts_mov_b32 %2, .Lgqa_end-.Lgqa_beg" : "=s"(pc), "=s"(cb_), "=s"(ce_));
      const unsigned co = (jj * 8 + (lane & 7)) * 128;
      const char* a = lane < 8 ? ws + grp * 4096 + (lane & 1) * 4 : (const char*)(pc - cb_) + (co < ce_ ? co : 0u);
      if (lane < 16) asm volatile("s_mov_b32 m0, %0\n\ts_nop 0\n\tglobal_load_lds_dword %1, off" :: "s"(ldsb + LDS_EP), "v"(a) : "memory");
    }
    rsrc_t dsrd = mk_srd(wqh + v * 128, DM * WQROW - h * DH * 2 - v * 128);
    unsigned dsoff = 64 * WQROW, dps = 8 * WQROW - PIECE, dss = 64 * WQROW, dmask = ~0u, dve = row_src(lane, WQROW);
    dma_half(ldsb + ring, dve, dsrd, 0, dps);
    dma_half(ldsb + ring + HALF, dve, dsrd, 32 * WQROW, dps);
    f32x4 acc[4][5];
    float mo[5];
    const int fsw = (lane >> 2) & 2;
    const lptr a01 = smem + ring + (lane >> 4) * PIECE + ((lane >> 2) & 3) * 128 + 8 * (lane & 3) + 32 * fsw, a23 = a01 + 64 - 64 * fsw;
    const lptr bx = smem + LDS_P + 16 * (lane ^ (2 * (lane >> 4))), by = smem + LDS_P + 5120 + ((16 * (lane ^ (2 * (lane >> 4)))) ^ 16);
    bf16x8 ya[4], yb[5];
#define AZERO() do { _Pragma("unroll") for (int u = 0; u < 4; ++u) _Pragma("unroll") for (int t = 0; t < 5; ++t) acc[u][t] = f32x4{0.f, 0.f, 0.f, 0.f}; \
                     _Pragma("unroll") for (int u = 0; u < 4; ++u) ya[u] = __builtin_bit_cast(bf16x8, u32x4{0, 0, 0, 0}); \
                     _Pragma("unroll") for (int t = 0; t < 5; ++t) yb[t] = __builtin_bit_cast(bf16x8, u32x4{0, 0, 0, 0}); } while (0)
    AZERO();
    MFMA_SETTLE();
#pragma unroll
    for (int t = 0; t < 5; ++t) mo[t] = NEG;
    SCHED_FENCE();
    dma_half(ldsb + ring + SLOT, dve, dsrd, 64 * WQROW, dps);
    int nsw = 5, nfl = 6;
    unsigned chm = 0;
    auto step = [&](int n, auto par_) {
      constexpr int PAR = decltype(par_)::value;
      WAIT_LGKM0(); BAR();
      WAIT_VM(8);
      bf16x8 xa[4], xb[5];
      u32x4 ch; unsigned rare;
      {
        const unsigned ddst = ldsb + ring + (1 - PAR) * SLOT + HALF, dso = dsoff + 4 * dps + HALF;
#define dma_piece_l(u, a, b, c, d) dma_piece<u>(a, b, c, d)
#define YDMA(u) dma_piece_l(u, ddst, dve, dsrd, dso + (u) * dps)
#define MM(S, u, t) mfma_ip(acc[u][t], S##a[u], S##b[t])
#define TRH(p) __builtin_amdgcn_ds_read_tr16_b64_v4i16((LAS s16x4*)(p))
#define CAT(x, y) __builtin_bit_cast(bf16x8, __builtin_shufflevector(x, y, 0, 1, 2, 3, 4, 5, 6, 7))
#define F SCHED_FENCE();
        s16x4 h0, h1, h2, h3;
        F MM(y, 0, 0); F ch = *(LAS u32x4*)(smem + LDS_CHG + (1 - PAR) * 16);
        F MM(y, 0, 1); F h0 = TRH(a01 + PAR * SLOT);
        F MM(y, 0, 2); F h1 = TRH(a01 + PAR * SLOT + 512);
        F MM(y, 0, 3); F h2 = TRH(a01 + PAR * SLOT + 32);
        F MM(y, 0, 4); F h3 = TRH(a01 + PAR * SLOT + 544); F YDMA(0);
        xa[0] = CAT(h0, h1); xa[1] = CAT(h2, h3);
        F MM(y, 1, 0); F xb[0] = ld128(bx + PAR * PBUF);
        F MM(y, 1, 1); F xb[1] = ld128(bx + PAR * PBUF + 1024);
        F MM(y, 1, 2); F xb[2] = ld128(bx + PAR * PBUF + 2048);
        F MM(y, 1, 3); F xb[3] = ld128(bx + PAR * PBUF + 3072);
        F MM(y, 1, 4); F xb[4] = ld128(bx + PAR * PBUF + 4096); F YDMA(1);
        F MM(y, 2, 0); F h0 = TRH(a23 + PAR * SLOT);
        F MM(y, 2, 1); F h1 = TRH(a23 + PAR * SLOT + 512);
        F MM(y, 2, 2); F h2 = TRH(a23 + PAR * SLOT + 32);
        F MM(y, 2, 3); F h3 = TRH(a23 + PAR * SLOT + 544);
        F MM(y, 2, 4); F YDMA(2);
        xa[2] = CAT(h0, h1); xa[3] = CAT(h2, h3);
        F MM(y, 3, 0); F MM(y, 3, 1); F
        rare = __builtin_amdgcn_readfirstlane(ch[0] | ch[1] | ch[2] | ch[3]) & chm;
        F MM(y, 3, 2); F YDMA(3);
        dsoff = (dsoff + dss) & dmask;
        F MM(y, 3, 3); F MM(y, 3, 4); F
#undef YDMA
      }
      if (PAR == 1 && UNLIKELY(n == nsw)) {
        if (n == 5) {
          dsrd = mk_srd(vbase + v * 128, wid == 7 ? 0 : SK * DH * 2 - v * 128);
          dsoff = rot; dmask = KVMASK; dps = 8 * DH * 2 - PIECE; dss = KVTILE; dve = row_src(opaque(lane), DH * 2);
          nsw = 21;
          STAMP(9);
        } else {
          dsrd = srdWo; dsoff = 0; dps = WOP - PIECE; dss = WOSLOT; dve = row_src(opaque(lane), DM * 2);
        }
      }
      if (UNLIKELY(rare)) {
        MFMA_SETTLE();
        WAIT_LGKM0(); BAR();
#pragma unroll
        for (int t = 0; t < 5; ++t) xb[t] = ld128(bx + PAR * PBUF + t * 1024);
        float rr[5][4];
        ld_refs(rr, smem + LDS_REF + (1 - PAR) * 1280 + (opaque(lane) & 15) * 4);
#pragma unroll
        for (int t = 0; t < 5; ++t) {
          float mn = mo[t];
#pragma unroll
          for (int ww = 0; ww < 4; ++ww) mn = fmaxf(mn, ch[ww] ? rr[t][ww] : NEG);
          const float fo = ex2(mo[t] - mn);
          mo[t] = mn;
#pragma unroll
          for (int u = 0; u < 4; ++u) acc[u][t] *= fo;
          SCHED_FENCE();
        }
        MFMA_SETTLE();
      }
      WAIT_VM(8); WAIT_LGKM0();
      {
        const unsigned ddst = ldsb + ring + PAR * SLOT;
#define XDMA(u) dma_piece_l(u, ddst, dve, dsrd, dsoff + (u) * dps)
        s16x4 h0, h1, h2, h3;
        F MM(x, 0, 0); F h0 = TRH(a01 + PAR * SLOT + HALF);
        F MM(x, 0, 1); F h1 = TRH(a01 + PAR * SLOT + HALF + 512);
        F MM(x, 0, 2); F h2 = TRH(a01 + PAR * SLOT + HALF + 32);
        F MM(x, 0, 3); F h3 = TRH(a01 + PAR * SLOT + HALF + 544);
        F MM(x, 0, 4); F XDMA(0);
        ya[0] = CAT(h0, h1); ya[1] = CAT(h2, h3);
        F MM(x, 1, 0); F yb[0] = ld128(by + PAR * PBUF);
        F MM(x, 1, 1); F yb[1] = ld128(by + PAR * PBUF + 1024);
        F MM(x, 1, 2); F yb[2] = ld128(by + PAR * PBUF + 2048);
        F MM(x, 1, 3); F yb[3] = ld128(by + PAR * PBUF + 3072);
        F MM(x, 1, 4); F yb[4] = ld128(by + PAR * PBUF + 4096); F XDMA(1);
        F MM(x, 2, 0); F h0 = TRH(a23 + PAR * SLOT + HALF);
        F MM(x, 2, 1); F h1 = TRH(a23 + PAR * SLOT + HALF + 512);
        F MM(x, 2, 2); F h2 = TRH(a23 + PAR * SLOT + HALF + 32);
        F MM(x, 2, 3); F h3 = TRH(a23 + PAR * SLOT + HALF + 544);
        F MM(x, 2, 4); F XDMA(2);
        ya[2] = CAT(h0, h1); ya[3] = CAT(h2, h3);
        F MM(x, 3, 0); F MM(x, 3, 1); F XDMA(3); F MM(x, 3, 2); F MM(x, 3, 3); F MM(x, 3, 4); F
#undef XDMA
      }
    };
#pragma unroll 1
    for (int n = 0;; n += 2) {
      step(n, IC<0>{});
      if (UNLIKELY(n == nfl)) {
        if (n == 22) STAMP(7);
        if (n == 6) STAMP(2);
        MFMA_SETTLE();
        if (wid == 7) WAIT_VM(0);
        WAIT_LGKM0(); BAR();
        const int ln = opaque(lane);
        const int a0 = frag_wr(ln);
        const lptr xo = smem + LDS_X + (3 - v) * PBUF;
#pragma unroll
        for (int u = 0; u < 4; ++u)
#pragma unroll
          for (int t = 0; t < 5; ++t) *(LAS u32x2*)(xo + frag_wr_u(a0, u & 1, u >> 1) + t * 1024) = pk4(mfma(ya[u], yb[t], acc[u][t]));
        if (n == 6) STAMP(3);
        WAIT_LGKM0(); BAR();
        if (n == 22) break;
        AZERO();
        MFMA_SETTLE();
        BAR();
        STAMP(4);
        if (wid == 7) {
          dsrd = mk_srd(vbase + 3 * 128, SK * DH * 2 - 3 * 128);
          dma_half(ldsb + ring + SLOT, dve, dsrd, rot, dps);
          dma_half(ldsb + ring + SLOT + HALF, dve, dsrd, rot + KVTILE / 2, dps);
          dma_half(ldsb + ring, dve, dsrd, (rot + KVTILE) & KVMASK, dps);
        }
        float rr[5][4];
        ld_refs(rr, smem + LDS_REF + (ln & 15) * 4);
#pragma unroll
        for (int t = 0; t < 5; ++t) mo[t] = max2(max3(rr[t][0], rr[t][1], rr[t][2]), rr[t][3]);
        nfl = 22; chm = ~0u;
      }
      step(n + 1, IC<1>{});
    }
  }
  if (wid < 7) {
    const int lane = opaque(tid) & 63;
    const int fsw = (lane >> 2) & 2, trb = (lane >> 4) * PIECE + ((lane >> 2) & 3) * 128 + 8 * (lane & 3);
    const bool qk = wid < 4;
    const unsigned c0 = qk ? ring : ring + SLOT, c1 = qk ? ring + SLOT : ring, c2 = qk ? ring : ring - 4 * RING, c3 = c2 + SLOT;
    const unsigned we = row_src(lane, DM * 2);
    constexpr unsigned WPS = WOP - PIECE;
    bf16x8 wf[8][4];
#define WREAD(s_, slot) do { _Pragma("unroll") for (int ks = 0; ks < 2; ++ks) _Pragma("unroll") for (int u = 0; u < 4; ++u) \
      wf[2 * (s_) + ks][u] = ldtr(smem + (slot) + trb + ks * HALF + 32 * (u ^ fsw)); } while (0)
#define WDMA(s_, slot) do { dma_half(ldsb + (slot), we, srdWo, (s_) * WOSLOT, WPS); dma_half(ldsb + (slot) + HALF, we, srdWo, (s_) * WOSLOT + 4 * WOP, WPS); } while (0)
    if (!qk) { dma_half(ldsb + c1 + HALF, we, srdWo, WOSLOT + 4 * WOP, WPS); WDMA(2, c2); WDMA(3, c3); WAIT_VM(24); } else WAIT_VM(8);
    WREAD(0, c0);
    if (qk) { WAIT_LGKM0(); WDMA(2, c2); BAR(); WAIT_VM(8); } else WAIT_VM(16);
    WREAD(1, c1);
    float inv[5];
    {
      float lr[5][4];
      ld_refs(lr, smem + LDS_L + (lane & 15) * 4);
#pragma unroll
      for (int t = 0; t < 5; ++t) {
        const float ls = (lr[t][0] + lr[t][1]) + (lr[t][2] + lr[t][3]);
        const float r = __builtin_amdgcn_rcpf(ls);
        inv[t] = r * (2.0f - ls * r);
      }
    }
    if (qk) { WAIT_LGKM0(); WDMA(3, c3); }
    WAIT_VM(8);
    WREAD(2, c2);
    WAIT_VM(0);
    WREAD(3, c3);
#undef WDMA
#undef WREAD
    if (qk) { WAIT_LGKM0(); BAR(); }
    STAMP(8);
    const rsrc_t srdP = mk_srd(ws + WS_PART + ((size_t)(grp * NBLK + wid * 10) * HQ + h) * 1024, 9 * 16384 + 1024);
    const int b0 = 16 * (lane ^ (2 * (lane >> 4)));
    bf16x8 of[2][8];
#define OREAD(t) do { _Pragma("unroll") for (int ks = 0; ks < 8; ++ks) of[(t) & 1][ks] = ld128(smem + LDS_X + (3 - (ks >> 1)) * PBUF + ((ks & 1) * 5 + (t)) * 1024 + ((ks & 1) ? (b0 ^ 16) : b0)); } while (0)
    OREAD(0);
#pragma unroll
    for (int t = 0; t < 5; ++t) {
      if (t < 4) OREAD(t < 4 ? t + 1 : 0);
      f32x4 o[4];
#pragma unroll
      for (int u = 0; u < 4; ++u) o[u] = f32x4{0.f, 0.f, 0.f, 0.f};
#pragma unroll
      for (int ks = 0; ks < 8; ++ks)
#pragma unroll
        for (int u = 0; u < 4; ++u) o[u] = mfma(wf[ks][u], of[t & 1][ks], o[u]);
#pragma unroll
      for (int m = 0; m < 2; ++m) {
        const u32x2 a = pk4(o[2 * m] * inv[t]), c = pk4(o[2 * m + 1] * inv[t]);
        __builtin_amdgcn_raw_buffer_store_b128(u32x4{a[0], a[1], c[0], c[1]}, srdP, lane * 16 + (t * 2 + m) * 16384, 0, 16);
      }
      SCHED_FENCE();
    }
#undef OREAD
  }
  STAMP(10);
  {
    const int lane = opaque(tid) & 63;
    gu64* const ctl = (gu64*)(ws + grp * 4096);
    const unsigned long long tag = *(LAS unsigned long long*)(smem + LDS_EP) + 1;
    if (wid < 7) {
      WAIT_VM(0);
      STAMP(11);
      if (lane == 0) __hip_atomic_store(ctl + 16 * (1 + wid) + h, tag, __ATOMIC_RELAXED, __HIP_MEMORY_SCOPE_AGENT);
    }
    const int blk = wid < 3 ? h + 16 * wid : wid == 7 ? 40 + h : 56 + h;
    if (wid < 3 ? blk < 40 : (wid == 7 || (wid == 3 && blk < NBLK))) {
      const int cb = blk / 10;
      const rsrc_t srd = mk_srd(ws + WS_PART + (size_t)(grp * NBLK + blk) * 16384, 16384);
      const gu64* fa = ctl + 16 * (1 + cb) + (lane & 15);
      const unsigned v0 = lane * 16, v1 = v0 + 4096, v2 = v0 + 8192, v3 = v0 + 12288;
      u32x4 pv[16]; unsigned long long fl; unsigned pend, nw, spin;
      if (wid == 7) __builtin_amdgcn_s_sleep(64);
#define FETCH(i, vo, off) "s_bitcmp1_b32 %[nw], " #i "\n\ts_cselect_b64 exec, -1, 0\n\tbuffer_load_dwordx4 %" #i ", %[" #vo "], %[srd], 0 offen offset:" #off " sc1\n\t"
      asm volatile("s_mov_b32 %[pend], 0xffff\n\ts_mov_b32 %[spin], 0x40000\n"
                   "1:\n\tglobal_load_dwordx2 %[fl], %[fa], off sc1\n\ts_waitcnt vmcnt(0)\n\tv_cmp_eq_u64 vcc, %[fl], %[tag]\n\ts_nop 0\n\t"
                   "s_and_b32 %[nw], vcc_lo, %[pend]\n\ts_cbranch_scc0 2f\n\t"
                   FETCH(0, v0, 0) FETCH(1, v0, 1024) FETCH(2, v0, 2048) FETCH(3, v0, 3072) FETCH(4, v1, 0) FETCH(5, v1, 1024) FETCH(6, v1, 2048) FETCH(7, v1, 3072)
                   FETCH(8, v2, 0) FETCH(9, v2, 1024) FETCH(10, v2, 2048) FETCH(11, v2, 3072) FETCH(12, v3, 0) FETCH(13, v3, 1024) FETCH(14, v3, 2048) FETCH(15, v3, 3072)
                   "s_mov_b64 exec, -1\n\ts_andn2_b32 %[pend], %[pend], %[nw]\n\ts_cbranch_scc0 3f\n"
                   "2:\n\ts_sleep 4\n\ts_sub_u32 %[spin], %[spin], 1\n\ts_cbranch_scc0 1b\n"
                   "3:\n\ts_waitcnt vmcnt(0)"
                   : "=&v"(pv[0]), "=&v"(pv[1]), "=&v"(pv[2]), "=&v"(pv[3]), "=&v"(pv[4]), "=&v"(pv[5]), "=&v"(pv[6]), "=&v"(pv[7]),
                     "=&v"(pv[8]), "=&v"(pv[9]), "=&v"(pv[10]), "=&v"(pv[11]), "=&v"(pv[12]), "=&v"(pv[13]), "=&v"(pv[14]), "=&v"(pv[15]),
                     [fl] "=&v"(fl), [pend] "=&s"(pend), [nw] "=&s"(nw), [spin] "=&s"(spin)
                   : [fa] "v"(fa), [tag] "v"(tag), [srd] "s"(srd), [v0] "v"(v0), [v1] "v"(v1), [v2] "v"(v2), [v3] "v"(v3)
                   : "memory", "vcc", "scc");
#undef FETCH
      STAMP(12);
      if (h == 0 && wid == 0 && lane == 0) __hip_atomic_store(ctl, tag, __ATOMIC_RELAXED, __HIP_MEMORY_SCOPE_AGENT);
      float s[8];
#pragma unroll
      for (int k = 0; k < 8; ++k) s[k] = 0.f;
#pragma unroll
      for (int i = 0; i < 16; ++i)
#pragma unroll
        for (int k = 0; k < 4; ++k) { s[2 * k] += blo(pv[i][k]); s[2 * k + 1] += bhi(pv[i][k]); }
      const int t = (blk % 10) >> 1, m = blk & 1;
      char* dst = out + ((size_t)(b * SQ + t * 16 + (lane & 15)) * (2 * DM) + br * DM + cb * 64 + m * 32 + (lane >> 4) * 4) * 2;
      *(u32x2*)dst = u32x2{pk2(s[0], s[1]), pk2(s[2], s[3])};
      *(u32x2*)(dst + 32) = u32x2{pk2(s[4], s[5]), pk2(s[6], s[7])};
      STAMP(13);
    }
  }
  STAMP_FLUSH();
  asm volatile(".Lgqa_end:");
}

extern "C" void kernel_launch(void* const* d_in, const int* in_sizes, int n_in, void* d_out, int out_size, void* d_ws, size_t ws_size,
                              hipStream_t stream) {
  (void)in_sizes; (void)n_in; (void)out_size; (void)ws_size;
  gqa_main<<<256, 512, 0, stream>>>((const char*)d_in[0], (const char*)d_in[1], (const char*)d_in[2], (const char*)d_in[3], (const char*)d_in[4],
                                    (const char*)d_in[5], (const char*)d_in[6], (const char*)d_in[7], (const char*)d_in[8], (const char*)d_in[9],
                                    (char*)d_ws, (char*)d_out);
}
```

```cpp
#include <hip/hip_runtime.h>
#include <hip/hip_bf16.h>
#include <stdint.h>

typedef __bf16 bf16x8 __attribute__((ext_vector_type(8)));
typedef __bf16 bf16x2 __attribute__((ext_vector_type(2)));
typedef short s16x4 __attribute__((ext_vector_type(4)));
typedef float f32x4 __attribute__((ext_vector_type(4)));
typedef float f32x2 __attribute__((ext_vector_type(2)));
typedef unsigned u32x4 __attribute__((ext_vector_type(4)));
typedef unsigned u32x2 __attribute__((ext_vector_type(2)));
#define LAS __attribute__((address_space(3)))
#define GAS __attribute__((address_space(1)))
typedef LAS char* lptr;
typedef GAS unsigned long long gu64;
typedef __amdgpu_buffer_rsrc_t rsrc_t;

constexpr int NB = 8, SQ = 80, DM = 448, HQ = 16, HKV = 8, SK = 1024, DH = 256;
constexpr int NTILE = SK / 64;
constexpr int PIECE = 1056, HALF = 4 * PIECE, SLOT = 8 * PIECE, RING = 2 * SLOT;
constexpr int LDS_X = 7 * RING;
constexpr int LDS_P = LDS_X + 20480, PBUF = 10240;
constexpr int LDS_REF = LDS_X + 40960;
constexpr int LDS_CHG = LDS_REF + 2560;
constexpr int LDS_L = LDS_CHG + 32;
constexpr int LDS_EP = LDS_L + 1280;
constexpr int LDS_SCR = LDS_EP + 64;
constexpr int LDS_TOTAL = 163840;
static_assert(LDS_SCR + 256 <= LDS_TOTAL && LDS_EP % 16 == 0 && RING <= 20480, "lds");

constexpr float SCL = 0.0625f * 1.4426950408889634f;
constexpr float THR = 10.f;
constexpr float NEG = -1.0e30f;
constexpr int NBLK = 70;
#define EARLY 0
#define NREG 1
#define REG0 0
constexpr size_t WS_REG = (size_t)16 * NBLK * 16384;
constexpr size_t WS_PART = 1 << 20;
constexpr unsigned OOB = 0x7ffffff0u;

#define WAIT_VM(n) asm volatile("s_waitcnt vmcnt(" #n ")" ::: "memory")
#define WAIT_LGKM0() do { asm volatile("" ::: "memory"); __builtin_amdgcn_s_waitcnt(0xC07F); asm volatile("" ::: "memory"); } while (0)
#define UNLIKELY(x) __builtin_expect(!!(x), 0)
#define LIKELY(x) __builtin_expect(!!(x), 1)
#define SCHED_FENCE() __builtin_amdgcn_sched_barrier(0)
#define MIX5() do { _Pragma("unroll") for (int i_ = 0; i_ < 5; ++i_) { __builtin_amdgcn_sched_group_barrier(0x8, 1, 0); __builtin_amdgcn_sched_group_barrier(0x100, 1, 0); } } while (0)
#define BAR() do { SCHED_FENCE(); asm volatile("" ::: "memory"); __builtin_amdgcn_s_barrier(); asm volatile("" ::: "memory"); SCHED_FENCE(); } while (0)

#define FSTAMP(i) do {} while (0)
#define STAMP(i) do {} while (0)
#define STAMP_DECL do {} while (0)
#define STAMP_FLUSH() do {} while (0)

#define DMA_POL "nt"
template <int P>
__device__ __forceinline__ void dma_piece(unsigned m0v, unsigned voff, rsrc_t srd, unsigned soff) {
  if (P == 0) asm volatile("s_mov_b32 m0, %0\n\ts_nop 0\n\tbuffer_load_dwordx4 %1, %2, %3 offen " DMA_POL " lds" :: "s"(m0v), "v"(voff), "s"(srd), "s"(soff) : "memory");
  else asm volatile("buffer_load_dwordx4 %0, %1, %2 offen offset:%3 " DMA_POL " lds" :: "v"(voff), "s"(srd), "s"(soff), "i"(P * PIECE) : "memory");
}
__device__ __forceinline__ void dma_half(unsigned m0v, unsigned voff, rsrc_t srd, unsigned soff, unsigned ps) {
  dma_piece<0>(m0v, voff, srd, soff); dma_piece<1>(m0v, voff, srd, soff + ps); dma_piece<2>(m0v, voff, srd, soff + 2 * ps); dma_piece<3>(m0v, voff, srd, soff + 3 * ps);
}
__device__ __forceinline__ void touch(unsigned m0v, unsigned voff, rsrc_t srd) {
  asm volatile("s_mov_b32 m0, %0\n\ts_nop 0\n\tbuffer_load_dword %1, %2, 0 offen lds" :: "s"(m0v), "v"(voff), "s"(srd) : "memory");
}
__device__ __forceinline__ bf16x8 ld128(lptr p) { return *(LAS bf16x8*)p; }
__device__ __forceinline__ bf16x8 ldtr(lptr p) {
  s16x4 a = __builtin_amdgcn_ds_read_tr16_b64_v4i16((LAS s16x4*)p);
  s16x4 b = __builtin_amdgcn_ds_read_tr16_b64_v4i16((LAS s16x4*)(p + 512));
  return __builtin_bit_cast(bf16x8, __builtin_shufflevector(a, b, 0, 1, 2, 3, 4, 5, 6, 7));
}
__device__ __forceinline__ unsigned pk2(float a, float b) { f32x2 t = {a, b}; return __builtin_bit_cast(unsigned, __builtin_convertvector(t, bf16x2)); }
__device__ __forceinline__ u32x2 pk4(f32x4 v) { u32x2 r = {pk2(v[0], v[1]), pk2(v[2], v[3])}; return r; }
__device__ __forceinline__ float blo(unsigned u) { return __uint_as_float(u << 16); }
__device__ __forceinline__ float bhi(unsigned u) { return __uint_as_float(u & 0xffff0000u); }
__device__ __forceinline__ f32x4 mfma(bf16x8 a, bf16x8 b, f32x4 c) { return __builtin_amdgcn_mfma_f32_16x16x32_bf16(a, b, c, 0, 0, 0); }
__device__ __forceinline__ void mfma_ip(f32x4& c, bf16x8 a, bf16x8 b) { asm volatile("v_mfma_f32_16x16x32_bf16 %0, %1, %2, %0" : "+v"(c) : "v"(a), "v"(b)); }
#define MFMA_SETTLE() do { SCHED_FENCE(); asm volatile("s_nop 15" ::: "memory"); SCHED_FENCE(); } while (0)
__device__ __forceinline__ float ex2(float x) { return __builtin_amdgcn_exp2f(x); }
__device__ __forceinline__ float max2(float a, float b) { return __builtin_elementwise_maximum(a, b); }
__device__ __forceinline__ float max3(float a, float b, float c) { return max2(max2(a, b), c); }
__device__ __forceinline__ float max4(f32x4 v) { return max2(max3(v[0], v[1], v[2]), v[3]); }
__device__ __forceinline__ float red_max_g(float v) {
  auto a = __builtin_amdgcn_permlane16_swap(__float_as_uint(v), __float_as_uint(v), false, false);
  v = max2(__uint_as_float(a[0]), __uint_as_float(a[1]));
  auto b = __builtin_amdgcn_permlane32_swap(__float_as_uint(v), __float_as_uint(v), false, false);
  return max2(__uint_as_float(b[0]), __uint_as_float(b[1]));
}
__device__ __forceinline__ float red_sum_g(float v) {
  auto a = __builtin_amdgcn_permlane16_swap(__float_as_uint(v), __float_as_uint(v), false, false);
  v = __uint_as_float(a[0]) + __uint_as_float(a[1]);
  auto b = __builtin_amdgcn_permlane32_swap(__float_as_uint(v), __float_as_uint(v), false, false);
  return __uint_as_float(b[0]) + __uint_as_float(b[1]);
}
template <int V> struct IC { static constexpr int value = V; };
__device__ __forceinline__ int opaque(int v) { asm volatile("" : "+v"(v)); return v; }
__device__ __forceinline__ rsrc_t mk_srd(const void* p, unsigned bytes) { return __builtin_amdgcn_make_buffer_rsrc((void*)p, (short)0, (int)bytes, 0x00020000); }
__device__ __forceinline__ unsigned row_src(int lane, int stride) { const int srow = lane >> 3; return srow * stride + (((lane & 7) ^ (((srow >> 1) & 1) << 2)) * 16); }
__device__ __forceinline__ void ld_refs(float (*rr)[4], lptr rb) {
#pragma unroll
  for (int t = 0; t < 5; ++t)
#pragma unroll
    for (int ww = 0; ww < 4; ++ww) rr[t][ww] = *(LAS float*)(rb + ww * 320 + t * 64);
#pragma unroll
  for (int t = 0; t < 5; ++t) asm volatile("" : "+v"(rr[t][0]), "+v"(rr[t][1]), "+v"(rr[t][2]), "+v"(rr[t][3]));
}
__device__ __forceinline__ int frag_wr(int lane) { return 16 * ((lane & 15) ^ (2 * (lane >> 5))) + 256 * (lane >> 5) + 8 * ((lane >> 4) & 1); }
__device__ __forceinline__ int frag_wr_u(int a0, int ulo, int uhi) { return (a0 ^ (16 * (4 * ulo + uhi))) + 512 * ulo + 5120 * uhi; }

__global__ __launch_bounds__(512, 2)
void gqa_main(const char* __restrict__ xA, const char* __restrict__ kA, const char* __restrict__ vA, const char* __restrict__ wqA, const char* __restrict__ woA,
              const char* __restrict__ xB, const char* __restrict__ kB, const char* __restrict__ vB, const char* __restrict__ wqB, const char* __restrict__ woB,
              char* ws, char* out) {
  __shared__ __attribute__((aligned(16))) char smem_raw[LDS_TOTAL];
  asm volatile(".Lgqa_beg:");
  const lptr smem = (lptr)smem_raw;
  const unsigned ldsb = (unsigned)(uintptr_t)smem;
  const int tid = threadIdx.x;
  const int wid = __builtin_amdgcn_readfirstlane(tid >> 6);
  const int bid = blockIdx.x, xcd = bid & 7, jj = bid >> 3;
  const int br = xcd >> 2, b = jj >> 2, kvh = 2 * (xcd & 3) + ((jj >> 1) & 1), h = 2 * kvh + (jj & 1);
  const int grp = br * NB + b;
  const char* const xb = (br ? xB : xA) + (size_t)b * SQ * DM * 2;
  const char* const wqh = (br ? wqB : wqA) + h * DH * 2;
  const char* const woh = (br ? woB : woA) + (size_t)h * DH * DM * 2;
  const char* const vbase = (br ? vB : vA) + (size_t)(b * HKV + kvh) * SK * DH * 2;
  const rsrc_t srdWo = mk_srd(woh + wid * 128, wid < 7 ? DH * DM * 2 - wid * 128 : 0);
  constexpr unsigned WOSLOT = 64 * DM * 2, WOP = 8 * DM * 2, WQROW = HQ * DH * 2, KVTILE = 64 * DH * 2;
  const unsigned ring = wid * RING;
#define ROT 0
  const unsigned rot = ROT ? ((b * HKV + kvh + 5 * br) * 7 & 15) * KVTILE : 0u;
  constexpr unsigned KVMASK = 16 * KVTILE - 1;
  STAMP_DECL;
  STAMP(0);

  const int v = wid - 4;
  float lsum[5] = {0.f, 0.f, 0.f, 0.f, 0.f};

  if (wid < 4) {
    const int w = wid;
    const int lane = tid & 63;
    __builtin_amdgcn_s_setprio(1);
    const rsrc_t srdK = mk_srd((br ? kB : kA) + ((size_t)(b * HKV + kvh) * SK + w * 16) * (DH * 2) - 128, (SK - w * 16) * DH * 2 + 128);
    const unsigned kv = (lane >> 5) * 512 + (((lane & 31) ^ (lane >> 5)) * 16);
    constexpr unsigned KPS = 1024 - PIECE;
#define ABL_K srdK
    {
      u32x4 xr[7][3];
      const rsrc_t srdX = mk_srd(xb, SQ * DM * 2);
      const unsigned xo = (8 * w + (lane >> 3)) * (DM * 2) + (lane & 7) * 16;
#define XLOAD(j) do { _Pragma("unroll") for (int i = 0; i < 3; ++i) \
        asm volatile("buffer_load_dwordx4 %0, %1, %2, %3 offen offset:%4" : "=v"(xr[j][i]) : "v"(xo), "s"(srdX), "s"(i * 32 * DM * 2), "i"((j) * 128) : "memory"); } while (0)
#define XWAIT(j, n) asm volatile("s_waitcnt vmcnt(" #n ")" : "+v"(xr[j][0]), "+v"(xr[j][1]), "+v"(xr[j][2]) :: "memory")
#define XWRITE(j) do { *(LAS u32x4*)(xdst + ((j) & 1) * PBUF) = xr[j][0]; *(LAS u32x4*)(xdst + ((j) & 1) * PBUF + 2048) = xr[j][1]; \
                       if (w < 2) *(LAS u32x4*)(xdst + ((j) & 1) * PBUF + 4096) = xr[j][2]; } while (0)
#define KHALF(n) dma_half(ldsb + ring + (n) * HALF, kv, ABL_K, 128 + ((rot + ((n) >> 1) * KVTILE) & KVMASK) + ((n) & 1) * 4096, KPS)
      XLOAD(0); XLOAD(1);
      if (lane < 8) *(LAS unsigned*)(smem + LDS_CHG + lane * 4) = 0;
      const int xg = lane & 3, xk = (lane >> 2) & 1;
      const lptr xdst = smem + LDS_P + (xk * 5 + (w >> 1)) * 1024 + 16 * ((8 * (w & 1) + (lane >> 3) + 16 * xg) ^ (2 * xg + xk));
      XWAIT(0, 3); XWRITE(0);
      XWAIT(1, 0); XWRITE(1);
      STAMP(1);
      WAIT_LGKM0(); BAR();
      STAMP(2);
      XLOAD(2); XLOAD(3); XLOAD(4); XLOAD(5); XLOAD(6);
      {
        const int id = 512 + 8 * (4 * lane + w) + b;
        touch(ldsb + LDS_SCR, lane < 40 ? (unsigned)((id >> 2) * WQROW + (id & 3) * 128) : OOB, mk_srd(wqh, DM * WQROW - h * DH * 2));
      }
      BAR(); FSTAMP(3); XWAIT(2, 13); XWRITE(2); KHALF(0); WAIT_LGKM0();
      BAR(); FSTAMP(4); XWAIT(3, 14); XWRITE(3); KHALF(1); WAIT_LGKM0();
      BAR(); FSTAMP(5); XWAIT(4, 15); XWRITE(4); KHALF(2); WAIT_LGKM0();
      BAR(); FSTAMP(6); XWAIT(5, 16); XWRITE(5); KHALF(3); WAIT_LGKM0();
      BAR(); FSTAMP(7); XWAIT(6, 17); XWRITE(6); WAIT_LGKM0();
      BAR(); FSTAMP(8);
      BAR(); FSTAMP(9);
#undef XLOAD
#undef XWAIT
#undef XWRITE
#undef KHALF
    }
    const lptr ka0 = smem + ring + ((lane & 15) >> 1) * PIECE + (lane & 1) * 512 + (((lane >> 4) ^ (lane & 1)) * 16);
    const lptr pw0 = smem + LDS_P + frag_wr_u(frag_wr(lane), w & 1, w >> 1);
    bf16x8 qf[8][5];
    float ref[5], l[5];
#define KDMA(i, dst, vo, srd, so, ps) do { SCHED_FENCE(); dma_piece<(i) & 3>((dst) + ((i) >> 2) * HALF, vo, srd, (so) + (i) * (ps) + ((i) >> 2) * HALF); SCHED_FENCE(); } while (0)
    {
      bf16x8 kf[8];
      WAIT_VM(8);
#pragma unroll
      for (int i = 0; i < 8; ++i) kf[i] = ld128(ka0 + 64 * i);
      WAIT_LGKM0(); BAR();
      STAMP(3);
      const lptr qb0 = smem + opaque(LDS_X + 16 * (lane ^ (2 * (lane >> 4)))), qb1 = smem + opaque(LDS_X + ((16 * (lane ^ (2 * (lane >> 4)))) ^ 16));
      f32x4 s[5];
#pragma unroll
      for (int t = 0; t < 5; ++t) s[t] = f32x4{0.f, 0.f, 0.f, 0.f};
#define QREAD(ks) do { _Pragma("unroll") for (int t = 0; t < 5; ++t) qf[ks][t] = ld128((((ks) & 1) ? qb1 : qb0) + (3 - ((ks) >> 1)) * PBUF + (((ks) & 1) * 5 + t) * 1024); } while (0)
#define QSTEP(ks) do { if ((ks) < 6) QREAD((ks) < 6 ? (ks) + 2 : 0); \
        _Pragma("unroll") for (int t = 0; t < 5; ++t) s[t] = mfma(kf[ks], qf[ks][t], s[t]); \
        MIX5(); KDMA(ks, ldsb + ring, kv, srdK, 128 + ((rot + 2 * KVTILE) & KVMASK), KPS); } while (0)
      QREAD(0); QREAD(1);
      SCHED_FENCE();
      QSTEP(0); QSTEP(1); QSTEP(2); QSTEP(3); QSTEP(4); QSTEP(5); QSTEP(6); QSTEP(7);
#undef QSTEP
#undef QREAD
      const lptr rb = smem + opaque(LDS_REF + (lane & 15) * 4);
#pragma unroll
      for (int t = 0; t < 5; ++t) *(LAS float*)(rb + w * 320 + t * 64) = fmaxf(NEG, ceilf(red_max_g(max4(s[t])) * SCL));
      WAIT_LGKM0(); BAR();
      STAMP(4);
      float rr[5][4];
      ld_refs(rr, rb);
#pragma unroll
      for (int t = 0; t < 5; ++t) {
        ref[t] = max2(max3(rr[t][0], rr[t][1], rr[t][2]), rr[t][3]);
        f32x4 p;
#pragma unroll
        for (int r = 0; r < 4; ++r) p[r] = ex2(fmaf(s[t][r], SCL, -ref[t]));
        l[t] = (p[0] + p[1]) + (p[2] + p[3]);
        *(LAS u32x2*)(pw0 + PBUF + t * 1024) = pk4(p);
      }
      WAIT_LGKM0(); BAR();
      STAMP(5);
    }
    auto adopt = [&](int par) {
      const u32x4 ch = *(LAS u32x4*)(smem + LDS_CHG + par * 16);
      if (UNLIKELY(__builtin_amdgcn_readfirstlane(ch[0] | ch[1] | ch[2] | ch[3]))) {
        float rr[5][4];
        ld_refs(rr, smem + LDS_REF + par * 1280 + (opaque(lane) & 15) * 4);
#pragma unroll
        for (int t = 0; t < 5; ++t) {
          float m = ref[t];
#pragma unroll
          for (int ww = 0; ww < 4; ++ww) m = fmaxf(m, ch[ww] ? rr[t][ww] : NEG);
          const float f = ex2(ref[t] - m);
          l[t] *= f; ref[t] = m;
          LAS u32x2* pp = (LAS u32x2*)(pw0 + (par ^ 1) * PBUF + t * 1024);
          const u32x2 pv = *pp;
          *pp = u32x2{pk2(blo(pv[0]) * f, bhi(pv[0]) * f), pk2(blo(pv[1]) * f, bhi(pv[1]) * f)};
          SCHED_FENCE();
        }
        WAIT_LGKM0(); BAR();
      }
    };
    rsrc_t ksrd = srdK; unsigned ksoff = (rot + 3 * KVTILE) & KVMASK, kbias = 128, kps = KPS, kss = KVTILE, kve = kv;
#pragma unroll 1
    for (int T = 1; T < NTILE; ++T) {
      if (UNLIKELY(T == NTILE - 2)) {
        STAMP(6);
        ksrd = srdWo; ksoff = 0; kbias = 0; kps = WOP - PIECE; kss = WOSLOT; kve = row_src(opaque(lane), DM * 2);
      }
      WAIT_VM(8);
      const lptr ka = ka0 + (T & 1) * SLOT;
      bf16x8 kf[8];
#pragma unroll
      for (int i = 0; i < 8; ++i) kf[i] = ld128(ka + 64 * i);
      adopt((T + 1) & 1);
      WAIT_LGKM0();
      f32x4 s[5];
#pragma unroll
      for (int t = 0; t < 5; ++t) s[t] = f32x4{0.f, 0.f, 0.f, 0.f};
      const unsigned kdst = ldsb + ring + (T & 1) * SLOT;
#define KSTEP(i) do { _Pragma("unroll") for (int t = 0; t < 5; ++t) s[t] = mfma(kf[i], qf[i][t], s[t]); KDMA(i, kdst, kve, ksrd, ksoff + kbias, kps); } while (0)
      KSTEP(0); KSTEP(1); KSTEP(2); KSTEP(3); KSTEP(4); KSTEP(5); KSTEP(6); KSTEP(7);
#undef KSTEP
      ksoff = (ksoff + kss) & KVMASK;
      float e4[5];
#pragma unroll
      for (int t = 0; t < 5; ++t) e4[t] = fmaf(max4(s[t]), SCL, -ref[t]);
      const float mx = max3(max3(e4[0], e4[1], e4[2]), e4[3], e4[4]);
      unsigned changed = 0;
      if (UNLIKELY(__builtin_amdgcn_ballot_w64(mx > THR))) {
        changed = 1;
        const int ln = opaque(lane);
        const lptr rb = smem + LDS_REF + ((T & 1) * 4 + w) * 320 + (ln & 15) * 4;
#pragma unroll
        for (int t = 0; t < 5; ++t) {
          const float m = red_max_g(max4(s[t]));
          const float nr = fmaxf(ref[t], ceilf(m * SCL));
          l[t] *= ex2(ref[t] - nr); ref[t] = nr;
          *(LAS float*)(rb + t * 64) = nr;
          SCHED_FENCE();
        }
      }
      const lptr pw = pw0 + ((T + 1) & 1) * PBUF;
#pragma unroll
      for (int t = 0; t < 5; ++t) {
        f32x4 p;
#pragma unroll
        for (int r = 0; r < 4; ++r) p[r] = ex2(fmaf(s[t][r], SCL, -ref[t]));
        l[t] += (p[0] + p[1]) + (p[2] + p[3]);
        *(LAS u32x2*)(pw + t * 1024) = pk4(p);
      }
      *(LAS unsigned*)(smem + LDS_CHG + ((T & 1) * 4 + w) * 4) = changed;
      WAIT_LGKM0(); BAR();
    }
#undef KDMA
    STAMP(7);
    adopt(1);
#pragma unroll
    for (int t = 0; t < 5; ++t) lsum[t] = l[t];
  } else {
    const int lane = tid & 63;
    if (wid == 7) {
      unsigned long long pc; unsigned cb_, ce_;
      asm volatile("s_getpc_b64 %0\n.Lgqa_pc:\n\ts_mov_b32 %1, .Lgqa_pc-.Lgqa_beg\n\ts_mov_b32 %2, .Lgqa_end-.Lgqa_beg" : "=s"(pc), "=s"(cb_), "=s"(ce_));
      const unsigned co = (jj * 8 + (lane & 7)) * 128;
      const char* a = lane < 8 ? ws + grp * 4096 + (lane & 1) * 4 : (const char*)(pc - cb_) + (co < ce_ ? co : 0u);
      if (lane < 16) asm volatile("s_mov_b32 m0, %0\n\ts_nop 0\n\tglobal_load_lds_dword %1, off" :: "s"(ldsb + LDS_EP), "v"(a) : "memory");
    }
    rsrc_t dsrd = mk_srd(wqh + v * 128, DM * WQROW - h * DH * 2 - v * 128);
    unsigned dsoff = 64 * WQROW, dps = 8 * WQROW - PIECE, dss = 64 * WQROW, dmask = ~0u, dve = row_src(lane, WQROW);
    dma_half(ldsb + ring, dve, dsrd, 0, dps);
    dma_half(ldsb + ring + HALF, dve, dsrd, 32 * WQROW, dps);
    f32x4 acc[4][5];
    float mo[5];
    const int fsw = (lane >> 2) & 2;
    const lptr a01 = smem + ring + (lane >> 4) * PIECE + ((lane >> 2) & 3) * 128 + 8 * (lane & 3) + 32 * fsw, a23 = a01 + 64 - 64 * fsw;
    const lptr bx = smem + LDS_P + 16 * (lane ^ (2 * (lane >> 4))), by = smem + LDS_P + 5120 + ((16 * (lane ^ (2 * (lane >> 4)))) ^ 16);
    bf16x8 ya[4], yb[5];
#define AZERO() do { _Pragma("unroll") for (int u = 0; u < 4; ++u) _Pragma("unroll") for (int t = 0; t < 5; ++t) acc[u][t] = f32x4{0.f, 0.f, 0.f, 0.f}; \
                     _Pragma("unroll") for (int u = 0; u < 4; ++u) ya[u] = __builtin_bit_cast(bf16x8, u32x4{0, 0, 0, 0}); \
                     _Pragma("unroll") for (int t = 0; t < 5; ++t) yb[t] = __builtin_bit_cast(bf16x8, u32x4{0, 0, 0, 0}); } while (0)
    AZERO();
    MFMA_SETTLE();
#pragma unroll
    for (int t = 0; t < 5; ++t) mo[t] = NEG;
    SCHED_FENCE();
    dma_half(ldsb + ring + SLOT, dve, dsrd, 64 * WQROW, dps);
    int nsw = 5, nfl = 6;
    unsigned chm = 0;
    auto step = [&](int n, auto par_) {
      constexpr int PAR = decltype(par_)::value;
      WAIT_LGKM0(); BAR();
      WAIT_VM(8);
      bf16x8 xa[4], xb[5];
      u32x4 ch; unsigned rare;
      {
        const unsigned ddst = ldsb + ring + (1 - PAR) * SLOT + HALF, dso = dsoff + 4 * dps + HALF;
#define dma_piece_l(u, a, b, c, d) dma_piece<u>(a, b, c, d)
#define YDMA(u) dma_piece_l(u, ddst, dve, dsrd, dso + (u) * dps)
#define MM(S, u, t) mfma_ip(acc[u][t], S##a[u], S##b[t])
#define TRH(p) __builtin_amdgcn_ds_read_tr16_b64_v4i16((LAS s16x4*)(p))
#define CAT(x, y) __builtin_bit_cast(bf16x8, __builtin_shufflevector(x, y, 0, 1, 2, 3, 4, 5, 6, 7))
#define F SCHED_FENCE();
        s16x4 h0, h1, h2, h3;
        F MM(y, 0, 0); F ch = *(LAS u32x4*)(smem + LDS_CHG + (1 - PAR) * 16);
        F MM(y, 0, 1); F h0 = TRH(a01 + PAR * SLOT);
        F MM(y, 0, 2); F h1 = TRH(a01 + PAR * SLOT + 512);
        F MM(y, 0, 3); F h2 = TRH(a01 + PAR * SLOT + 32);
        F MM(y, 0, 4); F h3 = TRH(a01 + PAR * SLOT + 544); F YDMA(0);
        xa[0] = CAT(h0, h1); xa[1] = CAT(h2, h3);
        F MM(y, 1, 0); F xb[0] = ld128(bx + PAR * PBUF);
        F MM(y, 1, 1); F xb[1] = ld128(bx + PAR * PBUF + 1024);
        F MM(y, 1, 2); F xb[2] = ld128(bx + PAR * PBUF + 2048);
        F MM(y, 1, 3); F xb[3] = ld128(bx + PAR * PBUF + 3072);
        F MM(y, 1, 4); F xb[4] = ld128(bx + PAR * PBUF + 4096); F YDMA(1);
        F MM(y, 2, 0); F h0 = TRH(a23 + PAR * SLOT);
        F MM(y, 2, 1); F h1 = TRH(a23 + PAR * SLOT + 512);
        F MM(y, 2, 2); F h2 = TRH(a23 + PAR * SLOT + 32);
        F MM(y, 2, 3); F h3 = TRH(a23 + PAR * SLOT + 544);
        F MM(y, 2, 4); F YDMA(2);
        xa[2] = CAT(h0, h1); xa[3] = CAT(h2, h3);
        F MM(y, 3, 0); F MM(y, 3, 1); F
        rare = __builtin_amdgcn_readfirstlane(ch[0] | ch[1] | ch[2] | ch[3]) & chm;
        F MM(y, 3, 2); F YDMA(3);
        dsoff = (dsoff + dss) & dmask;
        F MM(y, 3, 3); F MM(y, 3, 4); F
#undef YDMA
      }
      if (PAR == 1 && UNLIKELY(n == nsw)) {
        if (n == 5) {
          dsrd = mk_srd(vbase + v * 128, wid == 7 ? 0 : SK * DH * 2 - v * 128);
          dsoff = rot; dmask = KVMASK; dps = 8 * DH * 2 - PIECE; dss = KVTILE; dve = row_src(opaque(lane), DH * 2);
          nsw = 21;
          STAMP(9);
        } else {
          dsrd = srdWo; dsoff = 0; dps = WOP - PIECE; dss = WOSLOT; dve = row_src(opaque(lane), DM * 2);
        }
      }
      if (UNLIKELY(rare)) {
        MFMA_SETTLE();
        WAIT_LGKM0(); BAR();
#pragma unroll
        for (int t = 0; t < 5; ++t) xb[t] = ld128(bx + PAR * PBUF + t * 1024);
        float rr[5][4];
        ld_refs(rr, smem + LDS_REF + (1 - PAR) * 1280 + (opaque(lane) & 15) * 4);
#pragma unroll
        for (int t = 0; t < 5; ++t) {
          float mn = mo[t];
#pragma unroll
          for (int ww = 0; ww < 4; ++ww) mn = fmaxf(mn, ch[ww] ? rr[t][ww] : NEG);
          const float fo = ex2(mo[t] - mn);
          mo[t] = mn;
#pragma unroll
          for (int u = 0; u < 4; ++u) acc[u][t] *= fo;
          SCHED_FENCE();
        }
        MFMA_SETTLE();
      }
      WAIT_VM(8); WAIT_LGKM0();
      {
        const unsigned ddst = ldsb + ring + PAR * SLOT;
#define XDMA(u) dma_piece_l(u, ddst, dve, dsrd, dsoff + (u) * dps)
        s16x4 h0, h1, h2, h3;
        F MM(x, 0, 0); F h0 = TRH(a01 + PAR * SLOT + HALF);
        F MM(x, 0, 1); F h1 = TRH(a01 + PAR * SLOT + HALF + 512);
        F MM(x, 0, 2); F h2 = TRH(a01 + PAR * SLOT + HALF + 32);
        F MM(x, 0, 3); F h3 = TRH(a01 + PAR * SLOT + HALF + 544);
        F MM(x, 0, 4); F XDMA(0);
        ya[0] = CAT(h0, h1); ya[1] = CAT(h2, h3);
        F MM(x, 1, 0); F yb[0] = ld128(by + PAR * PBUF);
        F MM(x, 1, 1); F yb[1] = ld128(by + PAR * PBUF + 1024);
        F MM(x, 1, 2); F yb[2] = ld128(by + PAR * PBUF + 2048);
        F MM(x, 1, 3); F yb[3] = ld128(by + PAR * PBUF + 3072);
        F MM(x, 1, 4); F yb[4] = ld128(by + PAR * PBUF + 4096); F XDMA(1);
        F MM(x, 2, 0); F h0 = TRH(a23 + PAR * SLOT + HALF);
        F MM(x, 2, 1); F h1 = TRH(a23 + PAR * SLOT + HALF + 512);
        F MM(x, 2, 2); F h2 = TRH(a23 + PAR * SLOT + HALF + 32);
        F MM(x, 2, 3); F h3 = TRH(a23 + PAR * SLOT + HALF + 544);
        F MM(x, 2, 4); F XDMA(2);
        ya[2] = CAT(h0, h1); ya[3] = CAT(h2, h3);
        F MM(x, 3, 0); F MM(x, 3, 1); F XDMA(3); F MM(x, 3, 2); F MM(x, 3, 3); F MM(x, 3, 4); F
#undef XDMA
      }
    };
#pragma unroll 1
    for (int n = 0;; n += 2) {
      step(n, IC<0>{});
      if (UNLIKELY(n == nfl)) {
        if (n == 22) STAMP(7);
        if (n == 6) STAMP(2);
        MFMA_SETTLE();
        if (wid == 7) WAIT_VM(0);
        WAIT_LGKM0(); BAR();
        if (n == 22 && wid != 7) dma_half(ldsb + ring + HALF, dve, dsrd, dsoff + 4 * dps + HALF, dps);
        const int ln = opaque(lane);
        const int a0 = frag_wr(ln);
        lptr xo[4];
#pragma unroll
        for (int u = 0; u < 4; ++u) xo[u] = smem + opaque(LDS_X + (3 - v) * PBUF + frag_wr_u(a0, u & 1, u >> 1));
#pragma unroll
        for (int u = 0; u < 4; ++u)
#pragma unroll
          for (int t = 0; t < 5; ++t) *(LAS u32x2*)(xo[u] + t * 1024) = pk4(mfma(ya[u], yb[t], acc[u][t]));
        if (n == 6) STAMP(3);
        WAIT_LGKM0(); BAR();
        if (n == 22) break;
        AZERO();
        MFMA_SETTLE();
        BAR();
        STAMP(4);
        if (wid == 7) {
          dsrd = mk_srd(vbase + 3 * 128, SK * DH * 2 - 3 * 128);
          dma_half(ldsb + ring + SLOT, dve, dsrd, rot, dps);
          dma_half(ldsb + ring + SLOT + HALF, dve, dsrd, rot + KVTILE / 2, dps);
          dma_half(ldsb + ring, dve, dsrd, (rot + KVTILE) & KVMASK, dps);
        }
        touch(ldsb + LDS_SCR, ln < 56 ? (unsigned)((8 * (4 * ln + v) + b) * 128) : OOB, mk_srd(woh, DH * DM * 2));
        if (wid == 4) {
          const char* pb = ws + WS_PART + (REG0 + __builtin_amdgcn_readfirstlane(*(LAS unsigned*)(smem + LDS_EP)) % NREG) * WS_REG;
          const char* a = ln == 0 ? pb + (size_t)grp * NBLK * 16384 : ln == 1 ? pb + (size_t)(grp + 1) * NBLK * 16384 - 128 : out + (size_t)b * SQ * 4 * DM;
          if (ln < 3) asm volatile("s_mov_b32 m0, %0\n\ts_nop 0\n\tglobal_load_lds_dword %1, off" :: "s"(ldsb + LDS_SCR), "v"(a) : "memory");
        }
        float rr[5][4];
        ld_refs(rr, smem + LDS_REF + (ln & 15) * 4);
#pragma unroll
        for (int t = 0; t < 5; ++t) mo[t] = max2(max3(rr[t][0], rr[t][1], rr[t][2]), rr[t][3]);
        nfl = 22; chm = ~0u;
      }
      step(n + 1, IC<1>{});
    }
  }
  const char* const pbase = ws + WS_PART + (REG0 + __builtin_amdgcn_readfirstlane(*(LAS unsigned*)(smem + LDS_EP)) % NREG) * WS_REG;
  if (wid < 7) {
    const int lane = opaque(tid) & 63;
    const int fsw = (lane >> 2) & 2, trb = (lane >> 4) * PIECE + ((lane >> 2) & 3) * 128 + 8 * (lane & 3);
    const bool qk = wid < 4;
    const unsigned c0 = qk ? ring : ring + SLOT, c1 = qk ? ring + SLOT : ring, c2 = qk ? ring : ring - 4 * RING, c3 = c2 + SLOT;
    const unsigned we = row_src(lane, DM * 2);
    constexpr unsigned WPS = WOP - PIECE;
    bf16x8 wf[8][4];
#define WREAD(s_, slot) do { _Pragma("unroll") for (int ks = 0; ks < 2; ++ks) _Pragma("unroll") for (int u = 0; u < 4; ++u) \
      wf[2 * (s_) + ks][u] = ldtr(smem + (slot) + trb + ks * HALF + 32 * (u ^ fsw)); } while (0)
#define WDMA(s_, slot) do { dma_half(ldsb + (slot), we, srdWo, (s_) * WOSLOT, WPS); dma_half(ldsb + (slot) + HALF, we, srdWo, (s_) * WOSLOT + 4 * WOP, WPS); } while (0)
    if (!qk) { WDMA(2, c2); WDMA(3, c3); WAIT_VM(24); } else WAIT_VM(8);
    WREAD(0, c0);
    if (qk) { WAIT_LGKM0(); WDMA(2, c2); BAR(); WAIT_VM(8); } else WAIT_VM(16);
    WREAD(1, c1);
    if (qk) {
      WAIT_LGKM0(); WDMA(3, c3);
#pragma unroll
      for (int t = 0; t < 5; ++t) *(LAS float*)(smem + LDS_L + (wid * 80 + t * 16 + (lane & 15)) * 4) = red_sum_g(lsum[t]);
    }
    WAIT_VM(8);
    WREAD(2, c2);
    WAIT_VM(0);
    WREAD(3, c3);
#undef WDMA
#undef WREAD
    if (qk) { WAIT_LGKM0(); BAR(); }
    STAMP(8);
    const rsrc_t srdP = mk_srd(pbase + ((size_t)(grp * NBLK + wid * 10) * HQ + h) * 1024, 9 * 16384 + 1024);
    const lptr ob0 = smem + opaque(LDS_X + 16 * (lane ^ (2 * (lane >> 4)))), ob1 = smem + opaque(LDS_X + ((16 * (lane ^ (2 * (lane >> 4)))) ^ 16));
    bf16x8 of[2][8];
    f32x4 o[2][4];
    float inv[5];
#define OREAD(t) do { _Pragma("unroll") for (int ks = 0; ks < 8; ++ks) of[(t) & 1][ks] = ld128(((ks & 1) ? ob1 : ob0) + (3 - (ks >> 1)) * PBUF + ((ks & 1) * 5 + (t)) * 1024); } while (0)
#define OSTORE(t) do { _Pragma("unroll") for (int m = 0; m < 2; ++m) { \
        const u32x2 a = pk4(o[(t) & 1][2 * m] * inv[t]), c = pk4(o[(t) & 1][2 * m + 1] * inv[t]); \
        __builtin_amdgcn_raw_buffer_store_b128(u32x4{a[0], a[1], c[0], c[1]}, srdP, lane * 16 + ((t) * 2 + m) * 16384, 0, 16); } } while (0)
    OREAD(0);
    float lr[5][4];
    const lptr lb = smem + opaque(LDS_L + (lane & 15) * 4);
#pragma unroll
    for (int t = 0; t < 5; ++t)
#pragma unroll
      for (int ww = 0; ww < 4; ++ww) lr[t][ww] = *(LAS float*)(lb + ww * 320 + t * 64);
    SCHED_FENCE();
#pragma unroll
    for (int t = 0; t < 5; ++t) {
      if (t < 4) OREAD(t < 4 ? t + 1 : 0);
#pragma unroll
      for (int u = 0; u < 4; ++u) o[t & 1][u] = f32x4{0.f, 0.f, 0.f, 0.f};
#pragma unroll
      for (int ks = 0; ks < 8; ++ks)
#pragma unroll
        for (int u = 0; u < 4; ++u) o[t & 1][u] = mfma(wf[ks][u], of[t & 1][ks], o[t & 1][u]);
      if (t == 0) {
#pragma unroll
        for (int i = 0; i < 5; ++i) {
          const float ls = (lr[i][0] + lr[i][1]) + (lr[i][2] + lr[i][3]);
          const float r = __builtin_amdgcn_rcpf(ls);
          inv[i] = r * (2.0f - ls * r);
        }
      } else OSTORE(t > 0 ? t - 1 : 0);
#pragma unroll
      for (int i = 0; i < 32; ++i) {
        __builtin_amdgcn_sched_group_barrier(0x8, 1, 0);
        if (i < 8) __builtin_amdgcn_sched_group_barrier(0x100, 1, 0); else if (i < 14) __builtin_amdgcn_sched_group_barrier(0x2, 2, 0); else __builtin_amdgcn_sched_group_barrier(0x2, 1, 0);
        if (i == 19 || i == 31) __builtin_amdgcn_sched_group_barrier(0x40, 1, 0);
      }
      SCHED_FENCE();
    }
    OSTORE(4);
#undef OSTORE
#undef OREAD
  }
  STAMP(10);
  {
    const int lane = opaque(tid) & 63;
    gu64* const ctl = (gu64*)(ws + grp * 4096);
    const unsigned long long tag = *(LAS unsigned long long*)(smem + LDS_EP) + 1;
    if (wid < 7) {
      WAIT_VM(0);
      STAMP(11);
      if (lane == 0) __hip_atomic_store(ctl + 16 * (1 + wid) + h, tag, __ATOMIC_RELAXED, __HIP_MEMORY_SCOPE_AGENT);
    }
    const int blk = wid < 3 ? h + 16 * wid : wid == 7 ? 40 + h : 56 + h;
    if (wid < 3 ? blk < 40 : (wid == 7 || (wid == 3 && blk < NBLK))) {
      const int cb = blk / 10;
      const rsrc_t srd = mk_srd(pbase + (size_t)(grp * NBLK + blk) * 16384, 16384);
      const gu64* fa = ctl + 16 * (1 + cb) + (lane & 15);
      const unsigned v0 = lane * 16, v1 = v0 + 4096, v2 = v0 + 8192, v3 = v0 + 12288;
      u32x4 pv[16]; unsigned long long fl; unsigned pend, nw, spin;
      if (wid == 7) __builtin_amdgcn_s_sleep(64);
#pragma unroll 1
      for (unsigned spins = 0; spins < (1u << 18); ++spins) {
        if (__builtin_amdgcn_ballot_w64(__hip_atomic_load(fa, __ATOMIC_RELAXED, __HIP_MEMORY_SCOPE_AGENT) != tag) == 0) break;
        __builtin_amdgcn_s_sleep(4);
      }
      __builtin_amdgcn_fence(__ATOMIC_ACQUIRE, "wavefront");
#pragma unroll
      for (int i = 0; i < 16; ++i) pv[i] = __builtin_amdgcn_raw_buffer_load_b128(srd, v0 + i * 1024, 0, 16);
      STAMP(12);
      if (h == 0 && wid == 0 && lane == 0) __hip_atomic_store(ctl, tag, __ATOMIC_RELAXED, __HIP_MEMORY_SCOPE_AGENT);
      float s[8];
#pragma unroll
      for (int k = 0; k < 8; ++k) s[k] = 0.f;
#pragma unroll
      for (int i = 0; i < 16; ++i)
#pragma unroll
        for (int k = 0; k < 4; ++k) { s[2 * k] += blo(pv[i][k]); s[2 * k + 1] += bhi(pv[i][k]); }
      const int t = (blk % 10) >> 1, m = blk & 1;
      char* dst = out + ((size_t)(b * SQ + t * 16 + (lane & 15)) * (2 * DM) + br * DM + cb * 64 + m * 32 + (lane >> 4) * 4) * 2;
      *(u32x2*)dst = u32x2{pk2(s[0], s[1]), pk2(s[2], s[3])};
      *(u32x2*)(dst + 32) = u32x2{pk2(s[4], s[5]), pk2(s[6], s[7])};
      STAMP(13);
    }
  }
  STAMP_FLUSH();
  asm volatile(".Lgqa_end:");
}

extern "C" void kernel_launch(void* const* d_in, const int* in_sizes, int n_in, void* d_out, int out_size, void* d_ws, size_t ws_size,
                              hipStream_t stream) {
  (void)in_sizes; (void)n_in; (void)out_size; (void)ws_size;
  gqa_main<<<256, 512, 0, stream>>>((const char*)d_in[0], (const char*)d_in[1], (const char*)d_in[2], (const char*)d_in[3], (const char*)d_in[4],
                                    (const char*)d_in[5], (const char*)d_in[6], (const char*)d_in[7], (const char*)d_in[8], (const char*)d_in[9],
                                    (char*)d_ws, (char*)d_out);
}
```
